```python
import torch

def _softmax(x, axis=-1):
    return torch.softmax(x, dim=axis)
def _gelu(x):
    return torch.nn.functional.gelu(x, approximate="tanh")
def _silu(x):
    return torch.nn.functional.silu(x)
def _sigmoid(x):
    return torch.sigmoid(x)
def _layernorm(x, axis=-1, eps=1e-5):
    m = x.mean(dim=axis, keepdim=True)
    v = x.var(dim=axis, keepdim=True, unbiased=False)
    return (x - m) / torch.sqrt(v + eps)
def _scatter_add(tgt, idx, src, axis=0):
    return tgt.clone().index_add_(axis, idx, src)
def _segment_sum(src, seg, n):
    out = torch.zeros((n,) + src.shape[1:], dtype=src.dtype)
    return out.index_add_(0, seg, src)
def _pad(x, pw):
    flat = tuple(v for pair in reversed(pw) for v in pair)
    return torch.nn.functional.pad(x, flat)

kernel_name = 'parallel_sum__spectral_fnet__dense_activation_relu__prompt'

def setup_inputs():

    _w = (lambda a: (lambda t: t.to(torch.bfloat16) if t.is_floating_point() else t)(torch.as_tensor(a)))
    def _A_setup():
        B, S, D = 1, 2048, 1024
        ns = torch.arange(S)[:, None] * torch.arange(S)[None, :]
        nd = torch.arange(D)[:, None] * torch.arange(D)[None, :]
        Ws_re = torch.cos(-2.0 * torch.pi * ns / S).to(float)
        Ws_im = torch.sin(-2.0 * torch.pi * ns / S).to(float)
        Wd_re = torch.cos(-2.0 * torch.pi * nd / D).to(float)
        Wd_im = torch.sin(-2.0 * torch.pi * nd / D).to(float)
        return {
            "x": _w((lambda *s: torch.randn(*s, dtype=torch.bfloat16) * 0.05)(B, S, D)),
            "dft_seq_re": _w(Ws_re),
            "dft_seq_im": _w(Ws_im),
            "dft_hid_re": _w(Wd_re),
            "dft_hid_im": _w(Wd_im),
        }
    _A_in = _A_setup()

    def _B_setup():
        B, S, D, Dff = 1, 2048, 1024, 3584
        return {
            "x": _w((lambda *s: torch.randn(*s, dtype=torch.bfloat16) * 0.05)(B, S, D)),
            "w1": _w((lambda *s: torch.randn(*s, dtype=torch.bfloat16) * 0.4)(D, Dff)),
            "w2": _w((lambda *s: torch.randn(*s, dtype=torch.bfloat16) * 0.4)(Dff, D)),
        }
    _B_in = _B_setup()

    return {
        "A_x": _A_in["x"],
        "A_dft_seq_re": _A_in["dft_seq_re"],
        "A_dft_seq_im": _A_in["dft_seq_im"],
        "A_dft_hid_re": _A_in["dft_hid_re"],
        "A_dft_hid_im": _A_in["dft_hid_im"],
        "B_x": _B_in["x"],
        "B_w1": _B_in["w1"],
        "B_w2": _B_in["w2"],
    }

def reference(A_x, A_dft_seq_re, A_dft_seq_im, A_dft_hid_re, A_dft_hid_im, B_x, B_w1, B_w2):
    def _A_ref(x, dft_seq_re, dft_seq_im, dft_hid_re, dft_hid_im):
        a = torch.einsum("bsd,ed->bse", x, dft_hid_re)
        b = torch.einsum("bsd,ed->bse", x, dft_hid_im)
        y = torch.einsum("ts,bse->bte", dft_seq_re, a) - torch.einsum("ts,bse->bte", dft_seq_im, b)
        return y

    def _B_ref(x, w1, w2):
        h = torch.einsum("bsd,df->bsf", x, w1)
        zero = torch.zeros_like(h)
        h = torch.maximum(h, zero)
        return torch.einsum("bsf,fd->bsd", h, w2)

    x = A_x
    xn = _layernorm(x, axis=-1)
    return x + _A_ref(xn, A_dft_seq_re, A_dft_seq_im, A_dft_hid_re, A_dft_hid_im) + _B_ref(xn, B_w1, B_w2)
```

```cpp
#include <hip/hip_runtime.h>
#include <hip/hip_bf16.h>
typedef unsigned short u16;
typedef short bf16x8 __attribute__((ext_vector_type(8)));
typedef float f32x4 __attribute__((ext_vector_type(4)));
typedef unsigned u32x4 __attribute__((ext_vector_type(4)));

__device__ __forceinline__ float bf2f(u16 b) { return __uint_as_float(((unsigned)b) << 16); }
__device__ __forceinline__ u16 f2bf(float f) { __hip_bfloat16 h = __float2bfloat16(f); return *(u16*)&h; }

constexpr int S = 2048, D = 1024, F = 3584, KT = 2 * D + F;

__global__ __launch_bounds__(256) void k_ln(const u16* __restrict__ x, u16* __restrict__ xn) {
  const int row = blockIdx.x * 4 + (threadIdx.x >> 6), lane = threadIdx.x & 63;
  const u32x4* p = (const u32x4*)(x + (size_t)row * D);
  u32x4 v0 = p[lane], v1 = p[lane + 64];
  float f[16];
  for (int i = 0; i < 4; ++i) { f[2*i] = __uint_as_float(v0[i] << 16); f[2*i+1] = __uint_as_float(v0[i] & 0xffff0000u);
                                f[8+2*i] = __uint_as_float(v1[i] << 16); f[8+2*i+1] = __uint_as_float(v1[i] & 0xffff0000u); }
  float s = 0; for (int i = 0; i < 16; ++i) s += f[i];
  for (int o = 32; o >= 1; o >>= 1) s += __shfl_xor(s, o);
  const float m = s * (1.0f / D);
  float q = 0; for (int i = 0; i < 16; ++i) { float d = f[i] - m; q += d * d; }
  for (int o = 32; o >= 1; o >>= 1) q += __shfl_xor(q, o);
  const float r = 1.0f / sqrtf(q * (1.0f / D) + 1e-5f);
  u32x4 o0, o1;
  for (int i = 0; i < 4; ++i) { o0[i] = (unsigned)f2bf((f[2*i] - m) * r) | ((unsigned)f2bf((f[2*i+1] - m) * r) << 16);
                                o1[i] = (unsigned)f2bf((f[8+2*i] - m) * r) | ((unsigned)f2bf((f[8+2*i+1] - m) * r) << 16); }
  u32x4* po = (u32x4*)(xn + (size_t)row * D);
  po[lane] = o0; po[lane + 64] = o1;
}

template <bool BT>
__device__ __forceinline__ void gemm_acc(f32x4 (&acc)[4][4], const u16* A, int lda, const u16* B, int ldb, int K, u16* sA, u16* sB) {
  const int tid = threadIdx.x, lane = tid & 63, wid = tid >> 6, wr = wid >> 1, wc = wid & 1;
  for (int k0 = 0; k0 < K; k0 += 32) {
    __syncthreads();
    for (int i = tid; i < 512; i += 256) { int r = i >> 2, c = (i & 3) * 8; *(u32x4*)(sA + r * 40 + c) = *(const u32x4*)(A + (size_t)r * lda + k0 + c); }
    if (BT) { for (int i = tid; i < 512; i += 256) { int r = i >> 2, c = (i & 3) * 8; *(u32x4*)(sB + r * 40 + c) = *(const u32x4*)(B + (size_t)r * ldb + k0 + c); } }
    else { for (int i = tid; i < 512; i += 256) { int k = i >> 4, c = (i & 15) * 8; u32x4 v = *(const u32x4*)(B + (size_t)(k0 + k) * ldb + c);
        for (int j = 0; j < 4; ++j) { sB[(c + 2*j) * 40 + k] = (u16)(v[j] & 0xffff); sB[(c + 2*j + 1) * 40 + k] = (u16)(v[j] >> 16); } } }
    __syncthreads();
    bf16x8 a[4], b[4];
    for (int m = 0; m < 4; ++m) a[m] = *(const bf16x8*)(sA + (wr * 64 + m * 16 + (lane & 15)) * 40 + (lane >> 4) * 8);
    for (int n = 0; n < 4; ++n) b[n] = *(const bf16x8*)(sB + (wc * 64 + n * 16 + (lane & 15)) * 40 + (lane >> 4) * 8);
    for (int m = 0; m < 4; ++m) for (int n = 0; n < 4; ++n) acc[m][n] = __builtin_amdgcn_mfma_f32_16x16x32_bf16(a[m], b[n], acc[m][n], 0, 0, 0);
  }
}

__global__ __launch_bounds__(256) void k_stage1(const u16* xn, const u16* wsre, const u16* wsim, const u16* w1, u16* T) {
  __shared__ __attribute__((aligned(16))) u16 sA[128 * 40], sB[128 * 40];
  const int bm = blockIdx.y, bn = blockIdx.x;
  f32x4 acc[4][4] = {};
  int mode = bn < 8 ? 0 : (bn < 16 ? 1 : 2);
  if (mode == 0) gemm_acc<false>(acc, wsre + (size_t)bm * 128 * S, S, xn + bn * 128, D, S, sA, sB);
  else if (mode == 1) gemm_acc<false>(acc, wsim + (size_t)bm * 128 * S, S, xn + (bn - 8) * 128, D, S, sA, sB);
  else gemm_acc<false>(acc, xn + (size_t)bm * 128 * D, D, w1 + (bn - 16) * 128, F, D, sA, sB);
  const int lane = threadIdx.x & 63, wid = threadIdx.x >> 6, wr = wid >> 1, wc = wid & 1;
  for (int m = 0; m < 4; ++m) for (int n = 0; n < 4; ++n) for (int j = 0; j < 4; ++j) {
    float v = acc[m][n][j]; if (mode == 1) v = -v; if (mode == 2) v = fmaxf(v, 0.f);
    T[(size_t)(bm * 128 + wr * 64 + m * 16 + (lane >> 4) * 4 + j) * KT + bn * 128 + wc * 64 + n * 16 + (lane & 15)] = f2bf(v);
  }
}

__global__ __launch_bounds__(256) void k_stage2(const u16* x, const u16* T, const u16* wdre, const u16* wdim, const u16* w2, u16* out) {
  __shared__ __attribute__((aligned(16))) u16 sA[128 * 40], sB[128 * 40];
  const int bm = blockIdx.y, bn = blockIdx.x;
  f32x4 acc[4][4] = {};
  const u16* Ta = T + (size_t)bm * 128 * KT;
  gemm_acc<true>(acc, Ta, KT, wdre + (size_t)bn * 128 * D, D, D, sA, sB);
  gemm_acc<true>(acc, Ta + D, KT, wdim + (size_t)bn * 128 * D, D, D, sA, sB);
  gemm_acc<false>(acc, Ta + 2 * D, KT, w2 + bn * 128, D, F, sA, sB);
  const int lane = threadIdx.x & 63, wid = threadIdx.x >> 6, wr = wid >> 1, wc = wid & 1;
  for (int m = 0; m < 4; ++m) for (int n = 0; n < 4; ++n) for (int j = 0; j < 4; ++j) {
    size_t o = (size_t)(bm * 128 + wr * 64 + m * 16 + (lane >> 4) * 4 + j) * D + bn * 128 + wc * 64 + n * 16 + (lane & 15);
    out[o] = f2bf(acc[m][n][j] + bf2f(x[o]));
  }
}

extern "C" void kernel_launch(void* const* d_in, const int* in_sizes, int n_in, void* d_out, int out_size, void* d_ws, size_t ws_size, hipStream_t stream) {
  const u16* x = (const u16*)d_in[0]; const u16* wsre = (const u16*)d_in[1]; const u16* wsim = (const u16*)d_in[2];
  const u16* wdre = (const u16*)d_in[3]; const u16* wdim = (const u16*)d_in[4]; const u16* w1 = (const u16*)d_in[6]; const u16* w2 = (const u16*)d_in[7];
  u16* xn = (u16*)d_ws; u16* T = xn + (size_t)S * D;
  k_ln<<<S / 4, 256, 0, stream>>>(x, xn);
  k_stage1<<<dim3(44, 16), 256, 0, stream>>>(xn, wsre, wsim, w1, T);
  k_stage2<<<dim3(8, 16), 256, 0, stream>>>(x, T, wdre, wdim, w2, (u16*)d_out);
}
```

```cpp
#include <hip/hip_runtime.h>
#define LAS __attribute__((address_space(3)))

extern "C" __global__ __launch_bounds__(256, 1) void k_all(const char* x, const char* wsre, const char* wsim, const char* wdre, const char* wdim,
                                                           const char* w1, const char* w2, char* ws, char* out) {
  __shared__ __attribute__((aligned(1024))) char smem[163840];
  asm volatile(
    "s_load_dwordx16 s[4:19], %[ka], 0x0\n\t"
    "s_load_dwordx2 s[20:21], %[ka], 0x40\n\t"
    "s_cmp_lt_u32 %[b], 128\n\t"
    "s_cbranch_scc1 Lp%=\n\t"
    "s_sub_u32 s31, %[b], 128\n\t"
    "s_lshl_b32 s98, s31, 15\n\t"
    "v_lshrrev_b32 v3, 4, v0\n\t"
    "v_and_b32 v4, 15, v0\n\t"
    "v_lshlrev_b32 v4, 4, v4\n\t"
    "v_lshl_add_u32 v3, v3, 11, v4\n\t"
    "v_add_u32 v7, s98, v3\n\t"
    "s_waitcnt lgkmcnt(0)\n\t"
    "global_load_dwordx4 v[32:35], v7, s[4:5] offset:0\n\t"
    "global_load_dwordx4 v[36:39], v7, s[4:5] offset:256\n\t"
    "global_load_dwordx4 v[40:43], v7, s[4:5] offset:512\n\t"
    "global_load_dwordx4 v[44:47], v7, s[4:5] offset:768\n\t"
    "global_load_dwordx4 v[48:51], v7, s[4:5] offset:1024\n\t"
    "global_load_dwordx4 v[52:55], v7, s[4:5] offset:1280\n\t"
    "global_load_dwordx4 v[56:59], v7, s[4:5] offset:1536\n\t"
    "global_load_dwordx4 v[60:63], v7, s[4:5] offset:1792\n\t"
    "s_add_u32 s28, s18, 46137344\n\t"
    "s_addc_u32 s29, s19, 0\n\t"
    "s_load_dword s27, s[28:29], 69632\n\t"
    "s_lshl_b32 s31, s31, 7\n\t"
    "v_mov_b32 v3, s31\n\t"
    "global_load_dword v28, v3, s[28:29] sc1\n\t"
    "v_lshrrev_b32 v3, 6, v0\n\t"
    "v_and_b32 v1, 63, v0\n\t"
    "v_readfirstlane_b32 s22, v3\n\t"
    "s_and_b32 s24, %[b], 1\n\t"
    "s_bfe_u32 s25, %[b], 0x20001\n\t"
    "s_lshr_b32 s26, %[b], 3\n\t"
    "s_lshl_b32 s23, s22, 12\n\t"
    "s_bfe_u32 s33, s26, 0x10003\n\t"
    "s_and_b32 s34, s26, 7\n\t"
    "s_and_b32 s30, %[b], 7\n\t"
    "s_lshl_b32 s30, s30, 1\n\t"
    "s_add_u32 s30, s30, s33\n\t"
    "s_lshl_b32 s30, s30, 8\n\t"
    "s_lshl_b32 s31, s34, 2\n\t"
    "s_add_u32 s30, s30, s31\n\t"
    "s_add_u32 s30, s30, 16512\n\t"
    "s_lshl_b32 s31, s25, 1\n\t"
    "s_add_u32 s33, s33, s31\n\t"
    "s_mul_i32 s87, s34, 0\n\t"
    "v_lshrrev_b32 v3, 4, v0\n\t"
    "v_and_b32 v4, 3, v3\n\t"
    "v_lshl_add_u32 v3, s22, 4, v4\n\t"
    "s_movk_i32 s31, 7168\n\t"
    "v_mul_u32_u24 v3, s31, v3\n\t"
    "v_lshlrev_b32 v4, 1, v4\n\t"
    "v_and_b32 v5, 15, v0\n\t"
    "v_xor_b32 v4, v4, v5\n\t"
    "v_lshl_add_u32 v19, v4, 4, v3\n\t"
    "v_xor_b32 v20, 128, v19\n\t"
    "v_mov_b32 v6, 0x80000000\n\t"
    "v_cmp_gt_u32 vcc, 12, v4\n\t"
    "v_xor_b32 v4, 8, v4\n\t"
    "s_nop 0\n\t"
    "v_cndmask_b32 v21, v6, v19, vcc\n\t"
    "v_cmp_gt_u32 vcc, 12, v4\n\t"
    "s_nop 1\n\t"
    "v_cndmask_b32 v22, v6, v20, vcc\n\t"
    "s_lshl_b32 s31, s24, 3\n\t"
    "s_add_u32 s31, s31, s34\n\t"
    "s_mul_i32 s31, s31, 448\n\t"
    "s_add_u32 s38, s14, s31\n\t"
    "s_addc_u32 s39, s15, 0\n\t"
    "s_mov_b64 s[44:45], s[38:39]\n\t"
    "s_mov_b32 s46, 0x7fffffff\n\t"
    "s_mov_b32 s47, 0x00020000\n\t"
    "s_add_u32 s70, s23, 65536\n\t"
    "s_mov_b32 s77, 32768\n\t"
    "s_mov_b32 s60, 0\n\t"
    "s_mov_b32 s61, 27648\n\t"
    "s_mov_b32 s62, 55296\n\t"
    "s_mov_b32 s63, 82944\n\t"
    "s_mov_b32 s64, 256\n\t"
    "s_mov_b32 s65, 27904\n\t"
    "s_mov_b32 s66, 55552\n\t"
    "s_mov_b32 s67, 83200\n\t"
    "s_lshr_b32 s31, s34, 2\n\t"
    "s_mul_i32 s31, s31, 6\n\t"
    "s_add_u32 s31, s31, s34\n\t"
    "s_mul_i32 s35, s31, 524288\n\t"
    "s_mul_i32 s79, s34, 393216\n\t"
    "s_add_u32 s79, s79, 1572864\n\t"
    "s_and_b32 s31, s34, 1\n\t"
    "s_mul_i32 s98, s31, 192\n\t"
    "s_lshl_b32 s31, s31, 8\n\t"
    "s_add_u32 s60, s60, s98\n\t"
    "s_add_u32 s61, s61, s98\n\t"
    "s_add_u32 s62, s62, s98\n\t"
    "s_add_u32 s63, s63, s98\n\t"
    "s_sub_u32 s64, s64, s31\n\t"
    "s_sub_u32 s65, s65, s31\n\t"
    "s_sub_u32 s66, s66, s31\n\t"
    "s_sub_u32 s67, s67, s31\n\t"
    "s_mov_b32 s82, 0xffff0000\n\t"
    "s_waitcnt vmcnt(8)\n\t"
    "v_lshlrev_b32 v64, 16, v32\n\t"
    "v_and_b32 v65, s82, v32\n\t"
    "v_lshlrev_b32 v66, 16, v33\n\t"
    "v_and_b32 v67, s82, v33\n\t"
    "v_lshlrev_b32 v68, 16, v34\n\t"
    "v_and_b32 v69, s82, v34\n\t"
    "v_lshlrev_b32 v70, 16, v35\n\t"
    "v_and_b32 v71, s82, v35\n\t"
    "v_pk_add_f32 v[8:9], v[64:65], v[68:69]\n\t"
    "v_pk_add_f32 v[10:11], v[66:67], v[70:71]\n\t"
    "s_waitcnt vmcnt(7)\n\t"
    "v_lshlrev_b32 v72, 16, v36\n\t"
    "v_and_b32 v73, s82, v36\n\t"
    "v_lshlrev_b32 v74, 16, v37\n\t"
    "v_and_b32 v75, s82, v37\n\t"
    "v_lshlrev_b32 v76, 16, v38\n\t"
    "v_and_b32 v77, s82, v38\n\t"
    "v_lshlrev_b32 v78, 16, v39\n\t"
    "v_and_b32 v79, s82, v39\n\t"
    "v_pk_add_f32 v[8:9], v[8:9], v[72:73]\n\t"
    "v_pk_add_f32 v[10:11], v[10:11], v[74:75]\n\t"
    "v_pk_add_f32 v[8:9], v[8:9], v[76:77]\n\t"
    "v_pk_add_f32 v[10:11], v[10:11], v[78:79]\n\t"
    "s_waitcnt vmcnt(6)\n\t"
    "v_lshlrev_b32 v80, 16, v40\n\t"
    "v_and_b32 v81, s82, v40\n\t"
    "v_lshlrev_b32 v82, 16, v41\n\t"
    "v_and_b32 v83, s82, v41\n\t"
    "v_lshlrev_b32 v84, 16, v42\n\t"
    "v_and_b32 v85, s82, v42\n\t"
    "v_lshlrev_b32 v86, 16, v43\n\t"
    "v_and_b32 v87, s82, v43\n\t"
    "v_pk_add_f32 v[8:9], v[8:9], v[80:81]\n\t"
    "v_pk_add_f32 v[10:11], v[10:11], v[82:83]\n\t"
    "v_pk_add_f32 v[8:9], v[8:9], v[84:85]\n\t"
    "v_pk_add_f32 v[10:11], v[10:11], v[86:87]\n\t"
    "s_waitcnt vmcnt(5)\n\t"
    "v_lshlrev_b32 v88, 16, v44\n\t"
    "v_and_b32 v89, s82, v44\n\t"
    "v_lshlrev_b32 v90, 16, v45\n\t"
    "v_and_b32 v91, s82, v45\n\t"
    "v_lshlrev_b32 v92, 16, v46\n\t"
    "v_and_b32 v93, s82, v46\n\t"
    "v_lshlrev_b32 v94, 16, v47\n\t"
    "v_and_b32 v95, s82, v47\n\t"
    "v_pk_add_f32 v[8:9], v[8:9], v[88:89]\n\t"
    "v_pk_add_f32 v[10:11], v[10:11], v[90:91]\n\t"
    "v_pk_add_f32 v[8:9], v[8:9], v[92:93]\n\t"
    "v_pk_add_f32 v[10:11], v[10:11], v[94:95]\n\t"
    "s_waitcnt vmcnt(4)\n\t"
    "v_lshlrev_b32 v96, 16, v48\n\t"
    "v_and_b32 v97, s82, v48\n\t"
    "v_lshlrev_b32 v98, 16, v49\n\t"
    "v_and_b32 v99, s82, v49\n\t"
    "v_lshlrev_b32 v100, 16, v50\n\t"
    "v_and_b32 v101, s82, v50\n\t"
    "v_lshlrev_b32 v102, 16, v51\n\t"
    "v_and_b32 v103, s82, v51\n\t"
    "v_pk_add_f32 v[8:9], v[8:9], v[96:97]\n\t"
    "v_pk_add_f32 v[10:11], v[10:11], v[98:99]\n\t"
    "v_pk_add_f32 v[8:9], v[8:9], v[100:101]\n\t"
    "v_pk_add_f32 v[10:11], v[10:11], v[102:103]\n\t"
    "s_waitcnt vmcnt(3)\n\t"
    "v_lshlrev_b32 v104, 16, v52\n\t"
    "v_and_b32 v105, s82, v52\n\t"
    "v_lshlrev_b32 v106, 16, v53\n\t"
    "v_and_b32 v107, s82, v53\n\t"
    "v_lshlrev_b32 v108, 16, v54\n\t"
    "v_and_b32 v109, s82, v54\n\t"
    "v_lshlrev_b32 v110, 16, v55\n\t"
    "v_and_b32 v111, s82, v55\n\t"
    "v_pk_add_f32 v[8:9], v[8:9], v[104:105]\n\t"
    "v_pk_add_f32 v[10:11], v[10:11], v[106:107]\n\t"
    "v_pk_add_f32 v[8:9], v[8:9], v[108:109]\n\t"
    "v_pk_add_f32 v[10:11], v[10:11], v[110:111]\n\t"
    "s_waitcnt vmcnt(2)\n\t"
    "v_lshlrev_b32 v112, 16, v56\n\t"
    "v_and_b32 v113, s82, v56\n\t"
    "v_lshlrev_b32 v114, 16, v57\n\t"
    "v_and_b32 v115, s82, v57\n\t"
    "v_lshlrev_b32 v116, 16, v58\n\t"
    "v_and_b32 v117, s82, v58\n\t"
    "v_lshlrev_b32 v118, 16, v59\n\t"
    "v_and_b32 v119, s82, v59\n\t"
    "v_pk_add_f32 v[8:9], v[8:9], v[112:113]\n\t"
    "v_pk_add_f32 v[10:11], v[10:11], v[114:115]\n\t"
    "v_pk_add_f32 v[8:9], v[8:9], v[116:117]\n\t"
    "v_pk_add_f32 v[10:11], v[10:11], v[118:119]\n\t"
    "s_waitcnt vmcnt(1)\n\t"
    "v_lshlrev_b32 v120, 16, v60\n\t"
    "v_and_b32 v121, s82, v60\n\t"
    "v_lshlrev_b32 v122, 16, v61\n\t"
    "v_and_b32 v123, s82, v61\n\t"
    "v_lshlrev_b32 v124, 16, v62\n\t"
    "v_and_b32 v125, s82, v62\n\t"
    "v_lshlrev_b32 v126, 16, v63\n\t"
    "v_and_b32 v127, s82, v63\n\t"
    "v_pk_add_f32 v[8:9], v[8:9], v[120:121]\n\t"
    "v_pk_add_f32 v[10:11], v[10:11], v[122:123]\n\t"
    "v_pk_add_f32 v[8:9], v[8:9], v[124:125]\n\t"
    "v_pk_add_f32 v[10:11], v[10:11], v[126:127]\n\t"
    "v_pk_add_f32 v[8:9], v[8:9], v[10:11]\n\t"
    "v_add_f32 v8, v8, v9\n\t"
    "s_nop 1\n\t"
    "v_add_f32_dpp v8, v8, v8 quad_perm:[1,0,3,2] row_mask:0xf bank_mask:0xf\n\t"
    "s_nop 1\n\t"
    "v_add_f32_dpp v8, v8, v8 quad_perm:[2,3,0,1] row_mask:0xf bank_mask:0xf\n\t"
    "s_nop 1\n\t"
    "v_add_f32_dpp v8, v8, v8 row_half_mirror row_mask:0xf bank_mask:0xf\n\t"
    "s_nop 1\n\t"
    "v_add_f32_dpp v8, v8, v8 row_mirror row_mask:0xf bank_mask:0xf\n\t"
    "v_mul_f32 v12, 0xba800000, v8\n\t"
    "v_mov_b32 v13, v12\n\t"
    "v_pk_add_f32 v[64:65], v[64:65], v[12:13]\n\t"
    "v_pk_add_f32 v[66:67], v[66:67], v[12:13]\n\t"
    "v_pk_mul_f32 v[8:9], v[64:65], v[64:65]\n\t"
    "v_pk_add_f32 v[68:69], v[68:69], v[12:13]\n\t"
    "v_pk_mul_f32 v[10:11], v[66:67], v[66:67]\n\t"
    "v_pk_add_f32 v[70:71], v[70:71], v[12:13]\n\t"
    "v_pk_fma_f32 v[8:9], v[68:69], v[68:69], v[8:9]\n\t"
    "v_pk_add_f32 v[72:73], v[72:73], v[12:13]\n\t"
    "v_pk_fma_f32 v[10:11], v[70:71], v[70:71], v[10:11]\n\t"
    "v_pk_add_f32 v[74:75], v[74:75], v[12:13]\n\t"
    "v_pk_fma_f32 v[8:9], v[72:73], v[72:73], v[8:9]\n\t"
    "v_pk_add_f32 v[76:77], v[76:77], v[12:13]\n\t"
    "v_pk_fma_f32 v[10:11], v[74:75], v[74:75], v[10:11]\n\t"
    "v_pk_add_f32 v[78:79], v[78:79], v[12:13]\n\t"
    "v_pk_fma_f32 v[8:9], v[76:77], v[76:77], v[8:9]\n\t"
    "v_pk_add_f32 v[80:81], v[80:81], v[12:13]\n\t"
    "v_pk_fma_f32 v[10:11], v[78:79], v[78:79], v[10:11]\n\t"
    "v_pk_add_f32 v[82:83], v[82:83], v[12:13]\n\t"
    "v_pk_fma_f32 v[8:9], v[80:81], v[80:81], v[8:9]\n\t"
    "v_pk_add_f32 v[84:85], v[84:85], v[12:13]\n\t"
    "v_pk_fma_f32 v[10:11], v[82:83], v[82:83], v[10:11]\n\t"
    "v_pk_add_f32 v[86:87], v[86:87], v[12:13]\n\t"
    "v_pk_fma_f32 v[8:9], v[84:85], v[84:85], v[8:9]\n\t"
    "v_pk_add_f32 v[88:89], v[88:89], v[12:13]\n\t"
    "v_pk_fma_f32 v[10:11], v[86:87], v[86:87], v[10:11]\n\t"
    "v_pk_add_f32 v[90:91], v[90:91], v[12:13]\n\t"
    "v_pk_fma_f32 v[8:9], v[88:89], v[88:89], v[8:9]\n\t"
    "v_pk_add_f32 v[92:93], v[92:93], v[12:13]\n\t"
    "v_pk_fma_f32 v[10:11], v[90:91], v[90:91], v[10:11]\n\t"
    "v_pk_add_f32 v[94:95], v[94:95], v[12:13]\n\t"
    "v_pk_fma_f32 v[8:9], v[92:93], v[92:93], v[8:9]\n\t"
    "v_pk_add_f32 v[96:97], v[96:97], v[12:13]\n\t"
    "v_pk_fma_f32 v[10:11], v[94:95], v[94:95], v[10:11]\n\t"
    "v_pk_add_f32 v[98:99], v[98:99], v[12:13]\n\t"
    "v_pk_fma_f32 v[8:9], v[96:97], v[96:97], v[8:9]\n\t"
    "v_pk_add_f32 v[100:101], v[100:101], v[12:13]\n\t"
    "v_pk_fma_f32 v[10:11], v[98:99], v[98:99], v[10:11]\n\t"
    "v_pk_add_f32 v[102:103], v[102:103], v[12:13]\n\t"
    "v_pk_fma_f32 v[8:9], v[100:101], v[100:101], v[8:9]\n\t"
    "v_pk_add_f32 v[104:105], v[104:105], v[12:13]\n\t"
    "v_pk_fma_f32 v[10:11], v[102:103], v[102:103], v[10:11]\n\t"
    "v_pk_add_f32 v[106:107], v[106:107], v[12:13]\n\t"
    "v_pk_fma_f32 v[8:9], v[104:105], v[104:105], v[8:9]\n\t"
    "v_pk_add_f32 v[108:109], v[108:109], v[12:13]\n\t"
    "v_pk_fma_f32 v[10:11], v[106:107], v[106:107], v[10:11]\n\t"
    "v_pk_add_f32 v[110:111], v[110:111], v[12:13]\n\t"
    "v_pk_fma_f32 v[8:9], v[108:109], v[108:109], v[8:9]\n\t"
    "v_pk_add_f32 v[112:113], v[112:113], v[12:13]\n\t"
    "v_pk_fma_f32 v[10:11], v[110:111], v[110:111], v[10:11]\n\t"
    "v_pk_add_f32 v[114:115], v[114:115], v[12:13]\n\t"
    "v_pk_fma_f32 v[8:9], v[112:113], v[112:113], v[8:9]\n\t"
    "v_pk_add_f32 v[116:117], v[116:117], v[12:13]\n\t"
    "v_pk_fma_f32 v[10:11], v[114:115], v[114:115], v[10:11]\n\t"
    "v_pk_add_f32 v[118:119], v[118:119], v[12:13]\n\t"
    "v_pk_fma_f32 v[8:9], v[116:117], v[116:117], v[8:9]\n\t"
    "v_pk_add_f32 v[120:121], v[120:121], v[12:13]\n\t"
    "v_pk_fma_f32 v[10:11], v[118:119], v[118:119], v[10:11]\n\t"
    "v_pk_add_f32 v[122:123], v[122:123], v[12:13]\n\t"
    "v_pk_fma_f32 v[8:9], v[120:121], v[120:121], v[8:9]\n\t"
    "v_pk_add_f32 v[124:125], v[124:125], v[12:13]\n\t"
    "v_pk_fma_f32 v[10:11], v[122:123], v[122:123], v[10:11]\n\t"
    "v_pk_add_f32 v[126:127], v[126:127], v[12:13]\n\t"
    "v_pk_fma_f32 v[8:9], v[124:125], v[124:125], v[8:9]\n\t"
    "v_pk_fma_f32 v[10:11], v[126:127], v[126:127], v[10:11]\n\t"
    "v_pk_add_f32 v[8:9], v[8:9], v[10:11]\n\t"
    "v_add_f32 v8, v8, v9\n\t"
    "s_nop 1\n\t"
    "v_add_f32_dpp v8, v8, v8 quad_perm:[1,0,3,2] row_mask:0xf bank_mask:0xf\n\t"
    "s_nop 1\n\t"
    "v_add_f32_dpp v8, v8, v8 quad_perm:[2,3,0,1] row_mask:0xf bank_mask:0xf\n\t"
    "s_nop 1\n\t"
    "v_add_f32_dpp v8, v8, v8 row_half_mirror row_mask:0xf bank_mask:0xf\n\t"
    "s_nop 1\n\t"
    "v_add_f32_dpp v8, v8, v8 row_mirror row_mask:0xf bank_mask:0xf\n\t"
    "v_mov_b32 v13, 0x3727c5ac\n\t"
    "v_fmac_f32 v13, 0x3a800000, v8\n\t"
    "v_rsq_f32 v12, v13\n\t"
    "s_nop 0\n\t"
    "v_mov_b32 v13, v12\n\t"
    "v_pk_mul_f32 v[64:65], v[64:65], v[12:13]\n\t"
    "v_pk_mul_f32 v[66:67], v[66:67], v[12:13]\n\t"
    "v_pk_mul_f32 v[68:69], v[68:69], v[12:13]\n\t"
    "v_pk_mul_f32 v[70:71], v[70:71], v[12:13]\n\t"
    "v_cvt_pk_bf16_f32 v32, v64, v65\n\t"
    "v_cvt_pk_bf16_f32 v33, v66, v67\n\t"
    "v_cvt_pk_bf16_f32 v34, v68, v69\n\t"
    "v_cvt_pk_bf16_f32 v35, v70, v71\n\t"
    "global_store_dwordx4 v7, v[32:35], s[18:19] offset:0 sc1\n\t"
    "v_pk_mul_f32 v[72:73], v[72:73], v[12:13]\n\t"
    "v_pk_mul_f32 v[74:75], v[74:75], v[12:13]\n\t"
    "v_pk_mul_f32 v[76:77], v[76:77], v[12:13]\n\t"
    "v_pk_mul_f32 v[78:79], v[78:79], v[12:13]\n\t"
    "v_cvt_pk_bf16_f32 v36, v72, v73\n\t"
    "v_cvt_pk_bf16_f32 v37, v74, v75\n\t"
    "v_cvt_pk_bf16_f32 v38, v76, v77\n\t"
    "v_cvt_pk_bf16_f32 v39, v78, v79\n\t"
    "global_store_dwordx4 v7, v[36:39], s[18:19] offset:256 sc1\n\t"
    "v_pk_mul_f32 v[80:81], v[80:81], v[12:13]\n\t"
    "v_pk_mul_f32 v[82:83], v[82:83], v[12:13]\n\t"
    "v_pk_mul_f32 v[84:85], v[84:85], v[12:13]\n\t"
    "v_pk_mul_f32 v[86:87], v[86:87], v[12:13]\n\t"
    "v_cvt_pk_bf16_f32 v40, v80, v81\n\t"
    "v_cvt_pk_bf16_f32 v41, v82, v83\n\t"
    "v_cvt_pk_bf16_f32 v42, v84, v85\n\t"
    "v_cvt_pk_bf16_f32 v43, v86, v87\n\t"
    "global_store_dwordx4 v7, v[40:43], s[18:19] offset:512 sc1\n\t"
    "v_pk_mul_f32 v[88:89], v[88:89], v[12:13]\n\t"
    "v_pk_mul_f32 v[90:91], v[90:91], v[12:13]\n\t"
    "v_pk_mul_f32 v[92:93], v[92:93], v[12:13]\n\t"
    "v_pk_mul_f32 v[94:95], v[94:95], v[12:13]\n\t"
    "v_cvt_pk_bf16_f32 v44, v88, v89\n\t"
    "v_cvt_pk_bf16_f32 v45, v90, v91\n\t"
    "v_cvt_pk_bf16_f32 v46, v92, v93\n\t"
    "v_cvt_pk_bf16_f32 v47, v94, v95\n\t"
    "global_store_dwordx4 v7, v[44:47], s[18:19] offset:768 sc1\n\t"
    "v_pk_mul_f32 v[96:97], v[96:97], v[12:13]\n\t"
    "v_pk_mul_f32 v[98:99], v[98:99], v[12:13]\n\t"
    "v_pk_mul_f32 v[100:101], v[100:101], v[12:13]\n\t"
    "v_pk_mul_f32 v[102:103], v[102:103], v[12:13]\n\t"
    "v_cvt_pk_bf16_f32 v48, v96, v97\n\t"
    "v_cvt_pk_bf16_f32 v49, v98, v99\n\t"
    "v_cvt_pk_bf16_f32 v50, v100, v101\n\t"
    "v_cvt_pk_bf16_f32 v51, v102, v103\n\t"
    "global_store_dwordx4 v7, v[48:51], s[18:19] offset:1024 sc1\n\t"
    "v_pk_mul_f32 v[104:105], v[104:105], v[12:13]\n\t"
    "v_pk_mul_f32 v[106:107], v[106:107], v[12:13]\n\t"
    "v_pk_mul_f32 v[108:109], v[108:109], v[12:13]\n\t"
    "v_pk_mul_f32 v[110:111], v[110:111], v[12:13]\n\t"
    "v_cvt_pk_bf16_f32 v52, v104, v105\n\t"
    "v_cvt_pk_bf16_f32 v53, v106, v107\n\t"
    "v_cvt_pk_bf16_f32 v54, v108, v109\n\t"
    "v_cvt_pk_bf16_f32 v55, v110, v111\n\t"
    "global_store_dwordx4 v7, v[52:55], s[18:19] offset:1280 sc1\n\t"
    "v_pk_mul_f32 v[112:113], v[112:113], v[12:13]\n\t"
    "v_pk_mul_f32 v[114:115], v[114:115], v[12:13]\n\t"
    "v_pk_mul_f32 v[116:117], v[116:117], v[12:13]\n\t"
    "v_pk_mul_f32 v[118:119], v[118:119], v[12:13]\n\t"
    "v_cvt_pk_bf16_f32 v56, v112, v113\n\t"
    "v_cvt_pk_bf16_f32 v57, v114, v115\n\t"
    "v_cvt_pk_bf16_f32 v58, v116, v117\n\t"
    "v_cvt_pk_bf16_f32 v59, v118, v119\n\t"
    "global_store_dwordx4 v7, v[56:59], s[18:19] offset:1536 sc1\n\t"
    "v_pk_mul_f32 v[120:121], v[120:121], v[12:13]\n\t"
    "v_pk_mul_f32 v[122:123], v[122:123], v[12:13]\n\t"
    "v_pk_mul_f32 v[124:125], v[124:125], v[12:13]\n\t"
    "v_pk_mul_f32 v[126:127], v[126:127], v[12:13]\n\t"
    "v_cvt_pk_bf16_f32 v60, v120, v121\n\t"
    "v_cvt_pk_bf16_f32 v61, v122, v123\n\t"
    "v_cvt_pk_bf16_f32 v62, v124, v125\n\t"
    "v_cvt_pk_bf16_f32 v63, v126, v127\n\t"
    "global_store_dwordx4 v7, v[60:63], s[18:19] offset:1792 sc1\n\t"
    "s_waitcnt lgkmcnt(0)\n\t"
    "s_mul_i32 s27, s27, 0x9E3779B1\n\t"
    "s_add_u32 s27, s27, 0x7F4A7C15\n\t"
    "s_sub_u32 s31, %[b], 128\n\t"
    "s_lshl_b32 s31, s31, 7\n\t"
    "s_waitcnt vmcnt(0)\n\t"
    "s_barrier\n\t"
    "s_cmp_lg_u32 s22, 0\n\t"
    "s_cbranch_scc1 Loln%=\n\t"
    "v_mov_b32 v3, s31\n\t"
    "v_mov_b32 v4, s27\n\t"
    "s_mov_b64 exec, 1\n\t"
    "global_store_dword v3, v4, s[28:29] sc1\n\t"
    "s_mov_b64 exec, -1\n\t"
    "Loln%=:\n\t"
    "s_mov_b32 m0, s70\n\t"
    "s_nop 0\n\t"
    "buffer_load_dwordx4 v19, s[44:47], s60 offen offset:0 lds\n\t"
    "buffer_load_dwordx4 v19, s[44:47], s61 offen offset:1024 lds\n\t"
    "buffer_load_dwordx4 v20, s[44:47], s62 offen offset:2048 lds\n\t"
    "buffer_load_dwordx4 v20, s[44:47], s63 offen offset:3072 lds\n\t"
    "s_bitset1_b32 m0, 14\n\t"
    "s_nop 0\n\t"
    "buffer_load_dwordx4 v21, s[44:47], s64 offen offset:0 lds\n\t"
    "buffer_load_dwordx4 v21, s[44:47], s65 offen offset:1024 lds\n\t"
    "buffer_load_dwordx4 v22, s[44:47], s66 offen offset:2048 lds\n\t"
    "buffer_load_dwordx4 v22, s[44:47], s67 offen offset:3072 lds\n\t"
    "s_add_u32 s44, s44, 458752\n\t"
    "s_addc_u32 s45, s45, 0\n\t"
    "s_add_u32 s70, s70, s77\n\t"
    "s_mov_b32 s68, 16\n\t"
    "s_mov_b32 s76, -32768\n\t"
    "s_mov_b32 s71, -32768\n\t"
    "s_mov_b32 s73, 2\n\t"
    "s_mov_b32 s78, -65536\n\t"
    "s_mov_b32 s72, -65536\n\t"
    "s_mov_b32 s74, 3\n\t"
    "v_lshrrev_b32 v3, 3, v1\n\t"
    "v_and_b32 v4, 6, v3\n\t"
    "v_and_b32 v5, 7, v0\n\t"
    "v_xor_b32 v4, v4, v5\n\t"
    "v_lshl_add_u32 v3, s22, 5, v3\n\t"
    "v_lshlrev_b32 v3, 11, v3\n\t"
    "v_lshl_add_u32 v18, v4, 4, v3\n\t"
    "v_lshrrev_b32 v3, 4, v0\n\t"
    "v_and_b32 v4, 3, v3\n\t"
    "v_lshl_add_u32 v3, s22, 4, v4\n\t"
    "s_movk_i32 s31, 2048\n\t"
    "v_mul_u32_u24 v3, s31, v3\n\t"
    "v_lshlrev_b32 v4, 1, v4\n\t"
    "v_and_b32 v5, 15, v0\n\t"
    "v_xor_b32 v4, v4, v5\n\t"
    "v_lshl_add_u32 v2, v4, 4, v3\n\t"
    "s_lshl_b32 s31, s33, 19\n\t"
    "s_add_u32 s36, s18, s31\n\t"
    "s_addc_u32 s37, s19, 0\n\t"
    "s_mov_b64 s[40:41], s[36:37]\n\t"
    "s_mov_b32 s42, 0x7fffffff\n\t"
    "s_mov_b32 s43, 0x00020000\n\t"
    "s_add_u32 s69, s23, 0\n\t"
    "s_mov_b32 s75, 32768\n\t"
    "s_mov_b32 s52, 0\n\t"
    "s_mov_b32 s53, 15360\n\t"
    "s_mov_b32 s54, 30720\n\t"
    "s_mov_b32 s55, 46080\n\t"
    "s_mov_b32 s56, 262144\n\t"
    "s_mov_b32 s57, 277504\n\t"
    "s_mov_b32 s58, 292864\n\t"
    "s_mov_b32 s59, 308224\n\t"
    "v_lshrrev_b32 v3, 4, v1\n\t"
    "v_and_b32 v4, 6, v0\n\t"
    "v_xor_b32 v4, v4, v3\n\t"
    "v_lshlrev_b32 v4, 4, v4\n\t"
    "v_and_b32 v5, 15, v0\n\t"
    "v_lshl_add_u32 v4, v5, 7, v4\n\t"
    "s_lshl_b32 s31, s22, 13\n\t"
    "v_add_u32 v8, s31, v4\n\t"
    "v_xor_b32 v9, 64, v8\n\t"
    "v_bfe_u32 v4, v0, 2, 2\n\t"
    "v_lshl_add_u32 v3, v3, 3, v4\n\t"
    "v_lshlrev_b32 v3, 8, v3\n\t"
    "v_bfe_u32 v4, v0, 2, 3\n\t"
    "v_lshl_add_u32 v3, v4, 5, v3\n\t"
    "v_and_b32 v4, 3, v0\n\t"
    "v_lshl_add_u32 v3, v4, 3, v3\n\t"
    "v_add_u32 v10, 65536, v3\n\t"
    "v_xor_b32 v11, 32, v10\n\t"
    "v_xor_b32 v12, 64, v10\n\t"
    "v_xor_b32 v13, 96, v10\n\t"
    "v_xor_b32 v14, 128, v10\n\t"
    "v_xor_b32 v15, 160, v10\n\t"
    "v_xor_b32 v16, 192, v10\n\t"
    "v_xor_b32 v17, 224, v10\n\t"
    "s_mov_b32 s31, 0\n\t"
    "v_lshl_add_u32 v23, v1, 4, s23\n\t"
    "s_mul_i32 s98, s24, 88\n\t"
    "s_add_u32 s31, s31, s98\n\t"
    "s_mul_i32 s31, s31, 131072\n\t"
    "s_lshl_b32 s98, s33, 14\n\t"
    "s_add_u32 s31, s31, s98\n\t"
    "s_add_u32 s31, s31, 4194304\n\t"
    "s_add_u32 s48, s18, s31\n\t"
    "s_addc_u32 s49, s19, 0\n\t"
    "s_mov_b32 s50, 0x7fffffff\n\t"
    "s_mov_b32 s51, 0x00020000\n\t"
    "s_lshl_b32 s31, s25, 4\n\t"
    "s_and_b32 s98, s26, 15\n\t"
    "s_add_u32 s98, s31, s98\n\t"
    "s_lshl_b32 s98, s98, 2\n\t"
    "s_add_u32 s88, s98, s22\n\t"
    "s_lshl_b32 s31, s88, 15\n\t"
    "v_lshrrev_b32 v3, 2, v1\n\t"
    "v_lshl_add_u32 v3, v3, 12, s31\n\t"
    "v_and_b32 v4, 3, v0\n\t"
    "v_lshl_add_u32 v29, v4, 7, v3\n\t"
    "s_lshl_b32 s31, s88, 12\n\t"
    "v_lshl_add_u32 v6, v1, 7, s31\n\t"
    "v_mov_b32 v3, 0x80000000\n\t"
    "v_cmp_gt_u32 vcc, 32, v1\n\t"
    "s_nop 1\n\t"
    "v_cndmask_b32 v29, v3, v29, vcc\n\t"
    "v_cndmask_b32 v6, v3, v6, vcc\n\t"
    "s_mov_b32 s95, 0x00020000\n\t"
    "s_mov_b32 s86, 0\n\t"
    "s_bitcmp1_b32 %[b], 0\n\t"
    "s_cselect_b64 s[92:93], s[8:9], s[6:7]\n\t"
    "s_mov_b32 s94, 8388608\n\t"
    "s_mov_b32 s81, 1024\n\t"
    "s_mov_b32 s83, 512\n\t"
    "s_mov_b32 s85, 4096\n\t"
    "v_mov_b32 v7, v29\n\t"
    "v_mov_b64 v[60:61], 0\n\t"
    "v_mov_b64 v[62:63], 0\n\t"
    "v_mov_b64 v[116:117], 0\n\t"
    "v_mov_b64 v[118:119], 0\n\t"
    "v_mov_b64 v[172:173], 0\n\t"
    "v_mov_b64 v[174:175], 0\n\t"
    "v_mov_b64 v[228:229], 0\n\t"
    "v_mov_b64 v[230:231], 0\n\t"
    "v_mov_b64 v[232:233], 0\n\t"
    "v_mov_b64 v[234:235], 0\n\t"
    "v_mov_b64 v[176:177], 0\n\t"
    "v_mov_b64 v[178:179], 0\n\t"
    "v_mov_b64 v[120:121], 0\n\t"
    "v_mov_b64 v[122:123], 0\n\t"
    "v_mov_b64 v[64:65], 0\n\t"
    "v_mov_b64 v[66:67], 0\n\t"
    "v_mov_b64 v[68:69], 0\n\t"
    "v_mov_b64 v[70:71], 0\n\t"
    "v_mov_b64 v[124:125], 0\n\t"
    "v_mov_b64 v[126:127], 0\n\t"
    "v_mov_b64 v[180:181], 0\n\t"
    "v_mov_b64 v[182:183], 0\n\t"
    "v_mov_b64 v[236:237], 0\n\t"
    "v_mov_b64 v[238:239], 0\n\t"
    "v_mov_b64 v[240:241], 0\n\t"
    "v_mov_b64 v[242:243], 0\n\t"
    "v_mov_b64 v[184:185], 0\n\t"
    "v_mov_b64 v[186:187], 0\n\t"
    "v_mov_b64 v[128:129], 0\n\t"
    "v_mov_b64 v[130:131], 0\n\t"
    "v_mov_b64 v[72:73], 0\n\t"
    "v_mov_b64 v[74:75], 0\n\t"
    "v_mov_b64 v[76:77], 0\n\t"
    "v_mov_b64 v[78:79], 0\n\t"
    "v_mov_b64 v[132:133], 0\n\t"
    "v_mov_b64 v[134:135], 0\n\t"
    "v_mov_b64 v[188:189], 0\n\t"
    "v_mov_b64 v[190:191], 0\n\t"
    "v_mov_b64 v[244:245], 0\n\t"
    "v_mov_b64 v[246:247], 0\n\t"
    "v_mov_b64 v[248:249], 0\n\t"
    "v_mov_b64 v[250:251], 0\n\t"
    "v_mov_b64 v[192:193], 0\n\t"
    "v_mov_b64 v[194:195], 0\n\t"
    "v_mov_b64 v[136:137], 0\n\t"
    "v_mov_b64 v[138:139], 0\n\t"
    "v_mov_b64 v[80:81], 0\n\t"
    "v_mov_b64 v[82:83], 0\n\t"
    "v_mov_b64 v[84:85], 0\n\t"
    "v_mov_b64 v[86:87], 0\n\t"
    "v_mov_b64 v[140:141], 0\n\t"
    "v_mov_b64 v[142:143], 0\n\t"
    "v_mov_b64 v[196:197], 0\n\t"
    "v_mov_b64 v[198:199], 0\n\t"
    "v_mov_b64 v[252:253], 0\n\t"
    "v_mov_b64 v[254:255], 0\n\t"
    "v_mov_b64 v[224:225], 0\n\t"
    "v_mov_b64 v[226:227], 0\n\t"
    "s_cmp_lg_u32 s22, 0\n\t"
    "s_cbranch_scc1 Lsdlh%=\n\t"
    "v_and_b32 v5, 15, v0\n\t"
    "s_lshl_b32 s31, s33, 11\n\t"
    "v_lshl_add_u32 v5, v5, 7, s31\n\t"
    "s_mov_b32 s84, 0x20000\n\t"
    "Lsplh%=:\n\t"
    "global_load_dword v25, v5, s[28:29] offset:0 sc1\n\t"
    "s_waitcnt vmcnt(0)\n\t"
    "v_cmp_ne_u32 vcc, s27, v25\n\t"
    "s_sub_u32 s84, s84, 1\n\t"
    "s_cbranch_scc1 Lsdlh%=\n\t"
    "s_nop 2\n\t"
    "s_cbranch_vccz Lsdlh%=\n\t"
    "s_sleep 4\n\t"
    "s_branch Lsplh%=\n\t"
    "Lsdlh%=:\n\t"
    "s_barrier\n\t"
    "s_mov_b32 m0, s69\n\t"
    "s_nop 0\n\t"
    "buffer_load_dwordx4 v18, s[40:43], s52 offen offset:0 sc1 lds\n\t"
    "buffer_load_dwordx4 v18, s[40:43], s53 offen offset:1024 sc1 lds\n\t"
    "buffer_load_dwordx4 v18, s[40:43], s54 offen offset:2048 sc1 lds\n\t"
    "buffer_load_dwordx4 v18, s[40:43], s55 offen offset:3072 sc1 lds\n\t"
    "s_bitset1_b32 m0, 14\n\t"
    "s_nop 0\n\t"
    "buffer_load_dwordx4 v18, s[40:43], s56 offen offset:0 sc1 lds\n\t"
    "buffer_load_dwordx4 v18, s[40:43], s57 offen offset:1024 sc1 lds\n\t"
    "buffer_load_dwordx4 v18, s[40:43], s58 offen offset:2048 sc1 lds\n\t"
    "buffer_load_dwordx4 v18, s[40:43], s59 offen offset:3072 sc1 lds\n\t"
    "s_add_u32 s40, s40, 128\n\t"
    "s_addc_u32 s41, s41, 0\n\t"
    "s_add_u32 s69, s69, s75\n\t"
    "s_mov_b32 m0, s69\n\t"
    "s_nop 0\n\t"
    "buffer_load_dwordx4 v18, s[40:43], s52 offen offset:0 sc1 lds\n\t"
    "buffer_load_dwordx4 v18, s[40:43], s53 offen offset:1024 sc1 lds\n\t"
    "buffer_load_dwordx4 v18, s[40:43], s54 offen offset:2048 sc1 lds\n\t"
    "buffer_load_dwordx4 v18, s[40:43], s55 offen offset:3072 sc1 lds\n\t"
    "s_bitset1_b32 m0, 14\n\t"
    "s_nop 0\n\t"
    "buffer_load_dwordx4 v18, s[40:43], s56 offen offset:0 sc1 lds\n\t"
    "buffer_load_dwordx4 v18, s[40:43], s57 offen offset:1024 sc1 lds\n\t"
    "buffer_load_dwordx4 v18, s[40:43], s58 offen offset:2048 sc1 lds\n\t"
    "buffer_load_dwordx4 v18, s[40:43], s59 offen offset:3072 sc1 lds\n\t"
    "s_add_u32 s40, s40, 128\n\t"
    "s_addc_u32 s41, s41, 0\n\t"
    "s_add_u32 s69, s69, s71\n\t"
    "s_mov_b32 m0, s70\n\t"
    "s_nop 0\n\t"
    "buffer_load_dwordx4 v19, s[44:47], s60 offen offset:0 lds\n\t"
    "buffer_load_dwordx4 v19, s[44:47], s61 offen offset:1024 lds\n\t"
    "buffer_load_dwordx4 v20, s[44:47], s62 offen offset:2048 lds\n\t"
    "buffer_load_dwordx4 v20, s[44:47], s63 offen offset:3072 lds\n\t"
    "s_bitset1_b32 m0, 14\n\t"
    "s_nop 0\n\t"
    "buffer_load_dwordx4 v21, s[44:47], s64 offen offset:0 lds\n\t"
    "buffer_load_dwordx4 v21, s[44:47], s65 offen offset:1024 lds\n\t"
    "buffer_load_dwordx4 v22, s[44:47], s66 offen offset:2048 lds\n\t"
    "buffer_load_dwordx4 v22, s[44:47], s67 offen offset:3072 lds\n\t"
    "s_add_u32 s44, s44, 458752\n\t"
    "s_addc_u32 s45, s45, 0\n\t"
    "s_add_u32 s70, s70, s77\n\t"
    "s_waitcnt vmcnt(16)\n\t"
    "s_barrier\n\t"
    "ds_read_b128 a[0:3], v8 offset:0\n\t"
    "ds_read_b128 a[4:7], v8 offset:2048\n\t"
    "ds_read_b128 a[8:11], v8 offset:4096\n\t"
    "ds_read_b128 a[12:15], v8 offset:6144\n\t"
    "ds_read_b128 a[16:19], v9 offset:0\n\t"
    "ds_read_b128 a[20:23], v9 offset:2048\n\t"
    "ds_read_b128 a[24:27], v9 offset:4096\n\t"
    "ds_read_b128 a[28:31], v9 offset:6144\n\t"
    "ds_read_b64_tr_b16 a[64:65], v10 offset:0\n\t"
    "ds_read_b64_tr_b16 a[66:67], v10 offset:1024\n\t"
    "ds_read_b64_tr_b16 a[92:93], v10 offset:8192\n\t"
    "ds_read_b64_tr_b16 a[94:95], v10 offset:9216\n\t"
    "ds_read_b64_tr_b16 a[68:69], v11 offset:0\n\t"
    "ds_read_b64_tr_b16 a[70:71], v11 offset:1024\n\t"
    "ds_read_b64_tr_b16 a[96:97], v11 offset:8192\n\t"
    "ds_read_b64_tr_b16 a[98:99], v11 offset:9216\n\t"
    "ds_read_b64_tr_b16 a[72:73], v12 offset:0\n\t"
    "ds_read_b64_tr_b16 a[74:75], v12 offset:1024\n\t"
    "ds_read_b64_tr_b16 a[100:101], v12 offset:8192\n\t"
    "ds_read_b64_tr_b16 a[102:103], v12 offset:9216\n\t"
    "ds_read_b64_tr_b16 a[76:77], v13 offset:0\n\t"
    "ds_read_b64_tr_b16 a[78:79], v13 offset:1024\n\t"
    "ds_read_b64_tr_b16 a[104:105], v13 offset:8192\n\t"
    "ds_read_b64_tr_b16 a[106:107], v13 offset:9216\n\t"
    "ds_read_b64_tr_b16 a[80:81], v14 offset:0\n\t"
    "ds_read_b64_tr_b16 a[82:83], v14 offset:1024\n\t"
    "ds_read_b64_tr_b16 a[108:109], v14 offset:8192\n\t"
    "ds_read_b64_tr_b16 a[110:111], v14 offset:9216\n\t"
    "ds_read_b64_tr_b16 a[84:85], v15 offset:0\n\t"
    "ds_read_b64_tr_b16 a[86:87], v15 offset:1024\n\t"
    "ds_read_b64_tr_b16 a[112:113], v15 offset:8192\n\t"
    "ds_read_b64_tr_b16 a[114:115], v15 offset:9216\n\t"
    "ds_read_b64_tr_b16 a[88:89], v16 offset:0\n\t"
    "ds_read_b64_tr_b16 a[90:91], v16 offset:1024\n\t"
    "ds_read_b64_tr_b16 a[116:117], v16 offset:8192\n\t"
    "ds_read_b64_tr_b16 a[118:119], v16 offset:9216\n\t"
    "s_waitcnt lgkmcnt(0)\n\t"
    "v_mfma_f32_16x16x32_bf16 v[32:35], a[64:67], a[0:3], 0\n\t"
    "ds_read_b64_tr_b16 a[120:121], v17 offset:0\n\t"
    "s_mov_b32 m0, s70\n\t"
    "v_mfma_f32_16x16x32_bf16 v[32:35], a[92:95], a[16:19], v[32:35]\n\t"
    "buffer_load_dwordx4 v19, s[44:47], s60 offen offset:0 lds\n\t"
    "v_mfma_f32_16x16x32_bf16 v[88:91], a[92:95], a[20:23], 0\n\t"
    "ds_read_b64_tr_b16 a[122:123], v17 offset:1024\n\t"
    "v_mfma_f32_16x16x32_bf16 v[88:91], a[64:67], a[4:7], v[88:91]\n\t"
    "ds_read_b64_tr_b16 a[148:149], v17 offset:8192\n\t"
    "v_mfma_f32_16x16x32_bf16 v[144:147], a[64:67], a[8:11], 0\n\t"
    "ds_read_b64_tr_b16 a[150:151], v17 offset:9216\n\t"
    "v_mfma_f32_16x16x32_bf16 v[144:147], a[92:95], a[24:27], v[144:147]\n\t"
    "buffer_load_dwordx4 v19, s[44:47], s61 offen offset:1024 lds\n\t"
    "v_mfma_f32_16x16x32_bf16 v[200:203], a[92:95], a[28:31], 0\n\t"
    "ds_read_b64_tr_b16 a[124:125], v10 offset:16384\n\t"
    "v_mfma_f32_16x16x32_bf16 v[200:203], a[64:67], a[12:15], v[200:203]\n\t"
    "ds_read_b64_tr_b16 a[126:127], v10 offset:17408\n\t"
    "v_mfma_f32_16x16x32_bf16 v[204:207], a[68:71], a[12:15], 0\n\t"
    "ds_read_b64_tr_b16 a[152:153], v10 offset:24576\n\t"
    "v_mfma_f32_16x16x32_bf16 v[204:207], a[96:99], a[28:31], v[204:207]\n\t"
    "buffer_load_dwordx4 v20, s[44:47], s62 offen offset:2048 lds\n\t"
    "v_mfma_f32_16x16x32_bf16 v[148:151], a[96:99], a[24:27], 0\n\t"
    "ds_read_b64_tr_b16 a[154:155], v10 offset:25600\n\t"
    "v_mfma_f32_16x16x32_bf16 v[148:151], a[68:71], a[8:11], v[148:151]\n\t"
    "ds_read_b64_tr_b16 a[128:129], v11 offset:16384\n\t"
    "v_mfma_f32_16x16x32_bf16 v[92:95], a[68:71], a[4:7], 0\n\t"
    "ds_read_b64_tr_b16 a[130:131], v11 offset:17408\n\t"
    "v_mfma_f32_16x16x32_bf16 v[92:95], a[96:99], a[20:23], v[92:95]\n\t"
    "buffer_load_dwordx4 v20, s[44:47], s63 offen offset:3072 lds\n\t"
    "v_mfma_f32_16x16x32_bf16 v[36:39], a[96:99], a[16:19], 0\n\t"
    "ds_read_b64_tr_b16 a[156:157], v11 offset:24576\n\t"
    "v_mfma_f32_16x16x32_bf16 v[36:39], a[68:71], a[0:3], v[36:39]\n\t"
    "ds_read_b64_tr_b16 a[158:159], v11 offset:25600\n\t"
    "v_mfma_f32_16x16x32_bf16 v[40:43], a[72:75], a[0:3], 0\n\t"
    "ds_read_b64_tr_b16 a[132:133], v12 offset:16384\n\t"
    "s_bitset1_b32 m0, 14\n\t"
    "v_mfma_f32_16x16x32_bf16 v[40:43], a[100:103], a[16:19], v[40:43]\n\t"
    "buffer_load_dwordx4 v21, s[44:47], s64 offen offset:0 lds\n\t"
    "v_mfma_f32_16x16x32_bf16 v[96:99], a[100:103], a[20:23], 0\n\t"
    "ds_read_b64_tr_b16 a[134:135], v12 offset:17408\n\t"
    "v_mfma_f32_16x16x32_bf16 v[96:99], a[72:75], a[4:7], v[96:99]\n\t"
    "ds_read_b64_tr_b16 a[160:161], v12 offset:24576\n\t"
    "v_mfma_f32_16x16x32_bf16 v[152:155], a[72:75], a[8:11], 0\n\t"
    "ds_read_b64_tr_b16 a[162:163], v12 offset:25600\n\t"
    "v_mfma_f32_16x16x32_bf16 v[152:155], a[100:103], a[24:27], v[152:155]\n\t"
    "buffer_load_dwordx4 v21, s[44:47], s65 offen offset:1024 lds\n\t"
    "v_mfma_f32_16x16x32_bf16 v[208:211], a[100:103], a[28:31], 0\n\t"
    "ds_read_b64_tr_b16 a[136:137], v13 offset:16384\n\t"
    "v_mfma_f32_16x16x32_bf16 v[208:211], a[72:75], a[12:15], v[208:211]\n\t"
    "ds_read_b64_tr_b16 a[138:139], v13 offset:17408\n\t"
    "v_mfma_f32_16x16x32_bf16 v[212:215], a[76:79], a[12:15], 0\n\t"
    "ds_read_b64_tr_b16 a[164:165], v13 offset:24576\n\t"
    "v_mfma_f32_16x16x32_bf16 v[212:215], a[104:107], a[28:31], v[212:215]\n\t"
    "buffer_load_dwordx4 v22, s[44:47], s66 offen offset:2048 lds\n\t"
    "v_mfma_f32_16x16x32_bf16 v[156:159], a[104:107], a[24:27], 0\n\t"
    "ds_read_b64_tr_b16 a[166:167], v13 offset:25600\n\t"
    "v_mfma_f32_16x16x32_bf16 v[156:159], a[76:79], a[8:11], v[156:159]\n\t"
    "ds_read_b64_tr_b16 a[140:141], v14 offset:16384\n\t"
    "v_mfma_f32_16x16x32_bf16 v[100:103], a[76:79], a[4:7], 0\n\t"
    "ds_read_b64_tr_b16 a[142:143], v14 offset:17408\n\t"
    "v_mfma_f32_16x16x32_bf16 v[100:103], a[104:107], a[20:23], v[100:103]\n\t"
    "buffer_load_dwordx4 v22, s[44:47], s67 offen offset:3072 lds\n\t"
    "v_mfma_f32_16x16x32_bf16 v[44:47], a[104:107], a[16:19], 0\n\t"
    "ds_read_b64_tr_b16 a[168:169], v14 offset:24576\n\t"
    "s_add_u32 s44, s44, 458752\n\t"
    "v_mfma_f32_16x16x32_bf16 v[44:47], a[76:79], a[0:3], v[44:47]\n\t"
    "ds_read_b64_tr_b16 a[170:171], v14 offset:25600\n\t"
    "s_addc_u32 s45, s45, 0\n\t"
    "v_mfma_f32_16x16x32_bf16 v[48:51], a[80:83], a[0:3], 0\n\t"
    "ds_read_b64_tr_b16 a[144:145], v15 offset:16384\n\t"
    "s_add_u32 s70, s70, s72\n\t"
    "v_mfma_f32_16x16x32_bf16 v[48:51], a[108:111], a[16:19], v[48:51]\n\t"
    "ds_read_b64_tr_b16 a[146:147], v15 offset:17408\n\t"
    "v_mfma_f32_16x16x32_bf16 v[104:107], a[108:111], a[20:23], 0\n\t"
    "ds_read_b64_tr_b16 a[172:173], v15 offset:24576\n\t"
    "v_mfma_f32_16x16x32_bf16 v[104:107], a[80:83], a[4:7], v[104:107]\n\t"
    "ds_read_b64_tr_b16 a[174:175], v15 offset:25600\n\t"
    "v_mfma_f32_16x16x32_bf16 v[160:163], a[80:83], a[8:11], 0\n\t"
    "s_sub_u32 s73, s73, 1\n\t"
    "s_cmp_eq_u32 s73, 0\n\t"
    "v_mfma_f32_16x16x32_bf16 v[160:163], a[108:111], a[24:27], v[160:163]\n\t"
    "s_cselect_b32 s71, s76, s75\n\t"
    "v_mfma_f32_16x16x32_bf16 v[216:219], a[108:111], a[28:31], 0\n\t"
    "s_cselect_b32 s73, 2, s73\n\t"
    "v_mfma_f32_16x16x32_bf16 v[216:219], a[80:83], a[12:15], v[216:219]\n\t"
    "v_add_u32 v8, s71, v8\n\t"
    "v_mfma_f32_16x16x32_bf16 v[220:223], a[84:87], a[12:15], 0\n\t"
    "v_add_u32 v9, s71, v9\n\t"
    "v_mfma_f32_16x16x32_bf16 v[220:223], a[112:115], a[28:31], v[220:223]\n\t"
    "s_sub_u32 s74, s74, 1\n\t"
    "v_mfma_f32_16x16x32_bf16 v[164:167], a[112:115], a[24:27], 0\n\t"
    "s_cmp_eq_u32 s74, 0\n\t"
    "v_mfma_f32_16x16x32_bf16 v[164:167], a[84:87], a[8:11], v[164:167]\n\t"
    "s_cselect_b32 s72, s78, s77\n\t"
    "v_mfma_f32_16x16x32_bf16 v[108:111], a[84:87], a[4:7], 0\n\t"
    "s_cselect_b32 s74, 3, s74\n\t"
    "v_mfma_f32_16x16x32_bf16 v[108:111], a[112:115], a[20:23], v[108:111]\n\t"
    "v_add_u32 v10, s72, v10\n\t"
    "v_mfma_f32_16x16x32_bf16 v[52:55], a[112:115], a[16:19], 0\n\t"
    "v_add_u32 v11, s72, v11\n\t"
    "v_mfma_f32_16x16x32_bf16 v[52:55], a[84:87], a[0:3], v[52:55]\n\t"
    "v_add_u32 v12, s72, v12\n\t"
    "v_mfma_f32_16x16x32_bf16 v[56:59], a[88:91], a[0:3], 0\n\t"
    "v_add_u32 v13, s72, v13\n\t"
    "v_mfma_f32_16x16x32_bf16 v[56:59], a[116:119], a[16:19], v[56:59]\n\t"
    "v_add_u32 v14, s72, v14\n\t"
    "v_mfma_f32_16x16x32_bf16 v[112:115], a[116:119], a[20:23], 0\n\t"
    "v_add_u32 v15, s72, v15\n\t"
    "v_mfma_f32_16x16x32_bf16 v[112:115], a[88:91], a[4:7], v[112:115]\n\t"
    "v_add_u32 v16, s72, v16\n\t"
    "v_mfma_f32_16x16x32_bf16 v[168:171], a[88:91], a[8:11], 0\n\t"
    "v_add_u32 v17, s72, v17\n\t"
    "v_mfma_f32_16x16x32_bf16 v[168:171], a[116:119], a[24:27], v[168:171]\n\t"
    "s_sub_u32 s68, s68, 1\n\t"
    "s_branch Llooph%=\n\t"
    "Lea%=:\n\t"
    "s_mov_b64 exec, 1\n\t"
    "global_store_dword v3, v4, s[28:29] sc1\n\t"
    "s_mov_b64 exec, -1\n\t"
    "s_branch Leab%=\n\t"
    "Lt0%=:\n\t"
    "s_getpc_b64 s[96:97]\n\t"
    "Lr0%=:\n\t"
    "s_add_u32 s96, s96, Lt0b%=-Lr0%=\n\t"
    "s_addc_u32 s97, s97, 0\n\t"
    "s_branch Lseg%=\n\t"
    "Lt1%=:\n\t"
    "s_getpc_b64 s[96:97]\n\t"
    "Lr1%=:\n\t"
    "s_add_u32 s96, s96, Lt1b%=-Lr1%=\n\t"
    "s_addc_u32 s97, s97, 0\n\t"
    "s_branch Lseg%=\n\t"
    "Lseg%=:\n\t"
    "s_add_u32 s86, s86, 1\n\t"
    "s_cmp_lg_u32 s86, 1\n\t"
    "s_cbranch_scc1 Lsg0%=\n\t"
    "s_and_b32 s81, %[b], 1\n\t"
    "s_mul_i32 s81, s81, 1048576\n\t"
    "s_add_u32 s92, s14, s81\n\t"
    "s_addc_u32 s93, s15, 0\n\t"
    "s_mov_b32 s94, 6291456\n\t"
    "s_mov_b32 s81, 5242880\n\t"
    "s_mov_b32 s83, 1048576\n\t"
    "s_mov_b32 s85, 6291456\n\t"
    "v_mov_b32 v7, v6\n\t"
    "s_setpc_b64 s[96:97]\n\t"
    "Lsg0%=:\n\t"
    "s_cmp_lg_u32 s86, 2\n\t"
    "s_cbranch_scc1 Lsg1%=\n\t"
    "s_and_b32 s81, %[b], 1\n\t"
    "s_mul_i32 s81, s81, 3670016\n\t"
    "s_add_u32 s92, s16, s81\n\t"
    "s_addc_u32 s93, s17, 0\n\t"
    "s_mov_b32 s94, 3670016\n\t"
    "s_mov_b32 s81, 0\n\t"
    "s_mov_b32 s83, 1048576\n\t"
    "s_mov_b32 s85, 4194304\n\t"
    "v_mov_b32 v7, v6\n\t"
    "s_setpc_b64 s[96:97]\n\t"
    "Lsg1%=:\n\t"
    "s_cmp_lg_u32 s86, 3\n\t"
    "s_cbranch_scc1 Lsg2%=\n\t"
    "s_bitcmp1_b32 %[b], 0\n\t"
    "s_cselect_b64 s[92:93], s[12:13], s[10:11]\n\t"
    "s_mov_b32 s94, 2097152\n\t"
    "s_mov_b32 s81, 0\n\t"
    "s_mov_b32 s83, 1048576\n\t"
    "s_mov_b32 s85, 2097152\n\t"
    "v_mov_b32 v7, v6\n\t"
    "s_setpc_b64 s[96:97]\n\t"
    "Lsg2%=:\n\t"
    "s_mov_b32 s94, 0\n\t"
    "s_mov_b32 s85, -1\n\t"
    "s_setpc_b64 s[96:97]\n\t"
    "Llooph%=:\n\t"
    "v_mfma_f32_16x16x32_bf16 v[224:227], a[116:119], a[28:31], v[224:227]\n\t"
    "s_waitcnt vmcnt(8) lgkmcnt(0)\n\t"
    "v_mfma_f32_16x16x32_bf16 v[224:227], a[88:91], a[12:15], v[224:227]\n\t"
    "s_barrier\n\t"
    "v_mfma_f32_16x16x32_bf16 v[60:63], a[120:123], a[0:3], v[60:63]\n\t"
    "ds_read_b64_tr_b16 a[64:65], v10 offset:0\n\t"
    "v_mfma_f32_16x16x32_bf16 v[60:63], a[148:151], a[16:19], v[60:63]\n\t"
    "ds_read_b64_tr_b16 a[66:67], v10 offset:1024\n\t"
    "s_mov_b32 m0, s69\n\t"
    "v_mfma_f32_16x16x32_bf16 v[116:119], a[148:151], a[20:23], v[116:119]\n\t"
    "buffer_load_dwordx4 v18, s[40:43], s52 offen offset:0 sc1 lds\n\t"
    "v_mfma_f32_16x16x32_bf16 v[116:119], a[120:123], a[4:7], v[116:119]\n\t"
    "ds_read_b64_tr_b16 a[92:93], v10 offset:8192\n\t"
    "v_mfma_f32_16x16x32_bf16 v[172:175], a[120:123], a[8:11], v[172:175]\n\t"
    "ds_read_b64_tr_b16 a[94:95], v10 offset:9216\n\t"
    "v_mfma_f32_16x16x32_bf16 v[172:175], a[148:151], a[24:27], v[172:175]\n\t"
    "ds_read_b64_tr_b16 a[68:69], v11 offset:0\n\t"
    "v_mfma_f32_16x16x32_bf16 v[228:231], a[148:151], a[28:31], v[228:231]\n\t"
    "ds_read_b64_tr_b16 a[70:71], v11 offset:1024\n\t"
    "v_mfma_f32_16x16x32_bf16 v[228:231], a[120:123], a[12:15], v[228:231]\n\t"
    "buffer_load_dwordx4 v18, s[40:43], s53 offen offset:1024 sc1 lds\n\t"
    "v_mfma_f32_16x16x32_bf16 v[232:235], a[124:127], a[12:15], v[232:235]\n\t"
    "ds_read_b64_tr_b16 a[96:97], v11 offset:8192\n\t"
    "v_mfma_f32_16x16x32_bf16 v[232:235], a[152:155], a[28:31], v[232:235]\n\t"
    "ds_read_b64_tr_b16 a[98:99], v11 offset:9216\n\t"
    "v_mfma_f32_16x16x32_bf16 v[176:179], a[152:155], a[24:27], v[176:179]\n\t"
    "ds_read_b64_tr_b16 a[72:73], v12 offset:0\n\t"
    "v_mfma_f32_16x16x32_bf16 v[176:179], a[124:127], a[8:11], v[176:179]\n\t"
    "ds_read_b64_tr_b16 a[74:75], v12 offset:1024\n\t"
    "v_mfma_f32_16x16x32_bf16 v[120:123], a[124:127], a[4:7], v[120:123]\n\t"
    "ds_read_b64_tr_b16 a[100:101], v12 offset:8192\n\t"
    "v_mfma_f32_16x16x32_bf16 v[120:123], a[152:155], a[20:23], v[120:123]\n\t"
    "buffer_load_dwordx4 v18, s[40:43], s54 offen offset:2048 sc1 lds\n\t"
    "v_mfma_f32_16x16x32_bf16 v[64:67], a[152:155], a[16:19], v[64:67]\n\t"
    "ds_read_b64_tr_b16 a[102:103], v12 offset:9216\n\t"
    "v_mfma_f32_16x16x32_bf16 v[64:67], a[124:127], a[0:3], v[64:67]\n\t"
    "ds_read_b64_tr_b16 a[76:77], v13 offset:0\n\t"
    "v_mfma_f32_16x16x32_bf16 v[68:71], a[128:131], a[0:3], v[68:71]\n\t"
    "ds_read_b64_tr_b16 a[78:79], v13 offset:1024\n\t"
    "v_mfma_f32_16x16x32_bf16 v[68:71], a[156:159], a[16:19], v[68:71]\n\t"
    "ds_read_b64_tr_b16 a[104:105], v13 offset:8192\n\t"
    "v_mfma_f32_16x16x32_bf16 v[124:127], a[156:159], a[20:23], v[124:127]\n\t"
    "buffer_load_dwordx4 v18, s[40:43], s55 offen offset:3072 sc1 lds\n\t"
    "v_mfma_f32_16x16x32_bf16 v[124:127], a[128:131], a[4:7], v[124:127]\n\t"
    "ds_read_b64_tr_b16 a[106:107], v13 offset:9216\n\t"
    "v_mfma_f32_16x16x32_bf16 v[180:183], a[128:131], a[8:11], v[180:183]\n\t"
    "ds_read_b64_tr_b16 a[80:81], v14 offset:0\n\t"
    "v_mfma_f32_16x16x32_bf16 v[180:183], a[156:159], a[24:27], v[180:183]\n\t"
    "ds_read_b64_tr_b16 a[82:83], v14 offset:1024\n\t"
    "v_mfma_f32_16x16x32_bf16 v[236:239], a[156:159], a[28:31], v[236:239]\n\t"
    "ds_read_b64_tr_b16 a[108:109], v14 offset:8192\n\t"
    "v_mfma_f32_16x16x32_bf16 v[236:239], a[128:131], a[12:15], v[236:239]\n\t"
    "ds_read_b64_tr_b16 a[110:111], v14 offset:9216\n\t"
    "s_bitset1_b32 m0, 14\n\t"
    "v_mfma_f32_16x16x32_bf16 v[240:243], a[132:135], a[12:15], v[240:243]\n\t"
    "buffer_load_dwordx4 v18, s[40:43], s56 offen offset:0 sc1 lds\n\t"
    "v_mfma_f32_16x16x32_bf16 v[240:243], a[160:163], a[28:31], v[240:243]\n\t"
    "ds_read_b64_tr_b16 a[84:85], v15 offset:0\n\t"
    "v_mfma_f32_16x16x32_bf16 v[184:187], a[160:163], a[24:27], v[184:187]\n\t"
    "ds_read_b64_tr_b16 a[86:87], v15 offset:1024\n\t"
    "v_mfma_f32_16x16x32_bf16 v[184:187], a[132:135], a[8:11], v[184:187]\n\t"
    "ds_read_b64_tr_b16 a[112:113], v15 offset:8192\n\t"
    "v_mfma_f32_16x16x32_bf16 v[128:131], a[132:135], a[4:7], v[128:131]\n\t"
    "ds_read_b64_tr_b16 a[114:115], v15 offset:9216\n\t"
    "v_mfma_f32_16x16x32_bf16 v[128:131], a[160:163], a[20:23], v[128:131]\n\t"
    "buffer_load_dwordx4 v18, s[40:43], s57 offen offset:1024 sc1 lds\n\t"
    "v_mfma_f32_16x16x32_bf16 v[72:75], a[160:163], a[16:19], v[72:75]\n\t"
    "ds_read_b64_tr_b16 a[88:89], v16 offset:0\n\t"
    "v_mfma_f32_16x16x32_bf16 v[72:75], a[132:135], a[0:3], v[72:75]\n\t"
    "ds_read_b64_tr_b16 a[90:91], v16 offset:1024\n\t"
    "v_mfma_f32_16x16x32_bf16 v[76:79], a[136:139], a[0:3], v[76:79]\n\t"
    "ds_read_b64_tr_b16 a[116:117], v16 offset:8192\n\t"
    "v_mfma_f32_16x16x32_bf16 v[76:79], a[164:167], a[16:19], v[76:79]\n\t"
    "ds_read_b64_tr_b16 a[118:119], v16 offset:9216\n\t"
    "v_mfma_f32_16x16x32_bf16 v[132:135], a[164:167], a[20:23], v[132:135]\n\t"
    "ds_read_b128 a[32:35], v8 offset:0\n\t"
    "v_mfma_f32_16x16x32_bf16 v[132:135], a[136:139], a[4:7], v[132:135]\n\t"
    "buffer_load_dwordx4 v18, s[40:43], s58 offen offset:2048 sc1 lds\n\t"
    "v_mfma_f32_16x16x32_bf16 v[188:191], a[136:139], a[8:11], v[188:191]\n\t"
    "ds_read_b128 a[36:39], v8 offset:2048\n\t"
    "v_mfma_f32_16x16x32_bf16 v[188:191], a[164:167], a[24:27], v[188:191]\n\t"
    "ds_read_b128 a[40:43], v8 offset:4096\n\t"
    "v_mfma_f32_16x16x32_bf16 v[244:247], a[164:167], a[28:31], v[244:247]\n\t"
    "ds_read_b128 a[44:47], v8 offset:6144\n\t"
    "v_mfma_f32_16x16x32_bf16 v[244:247], a[136:139], a[12:15], v[244:247]\n\t"
    "ds_read_b128 a[48:51], v9 offset:0\n\t"
    "v_mfma_f32_16x16x32_bf16 v[248:251], a[140:143], a[12:15], v[248:251]\n\t"
    "buffer_load_dwordx4 v18, s[40:43], s59 offen offset:3072 sc1 lds\n\t"
    "v_mfma_f32_16x16x32_bf16 v[248:251], a[168:171], a[28:31], v[248:251]\n\t"
    "ds_read_b128 a[52:55], v9 offset:2048\n\t"
    "s_add_u32 s40, s40, 128\n\t"
    "v_mfma_f32_16x16x32_bf16 v[192:195], a[168:171], a[24:27], v[192:195]\n\t"
    "ds_read_b128 a[56:59], v9 offset:4096\n\t"
    "s_addc_u32 s41, s41, 0\n\t"
    "v_mfma_f32_16x16x32_bf16 v[192:195], a[140:143], a[8:11], v[192:195]\n\t"
    "ds_read_b128 a[60:63], v9 offset:6144\n\t"
    "s_add_u32 s69, s69, s71\n\t"
    "v_mfma_f32_16x16x32_bf16 v[136:139], a[140:143], a[4:7], v[136:139]\n\t"
    "v_mfma_f32_16x16x32_bf16 v[136:139], a[168:171], a[20:23], v[136:139]\n\t"
    "v_mfma_f32_16x16x32_bf16 v[80:83], a[168:171], a[16:19], v[80:83]\n\t"
    "v_mfma_f32_16x16x32_bf16 v[80:83], a[140:143], a[0:3], v[80:83]\n\t"
    "v_mfma_f32_16x16x32_bf16 v[84:87], a[144:147], a[0:3], v[84:87]\n\t"
    "v_mfma_f32_16x16x32_bf16 v[84:87], a[172:175], a[16:19], v[84:87]\n\t"
    "v_mfma_f32_16x16x32_bf16 v[140:143], a[172:175], a[20:23], v[140:143]\n\t"
    "v_mfma_f32_16x16x32_bf16 v[140:143], a[144:147], a[4:7], v[140:143]\n\t"
    "s_mov_b32 m0, s70\n\t"
    "v_mfma_f32_16x16x32_bf16 v[196:199], a[144:147], a[8:11], v[196:199]\n\t"
    "buffer_load_dwordx4 v19, s[44:47], s60 offen offset:0 lds\n\t"
    "v_mfma_f32_16x16x32_bf16 v[196:199], a[172:175], a[24:27], v[196:199]\n\t"
    "v_mfma_f32_16x16x32_bf16 v[252:255], a[172:175], a[28:31], v[252:255]\n\t"
    "v_mfma_f32_16x16x32_bf16 v[252:255], a[144:147], a[12:15], v[252:255]\n\t"
    "s_waitcnt lgkmcnt(0)\n\t"
    "v_mfma_f32_16x16x32_bf16 v[32:35], a[64:67], a[32:35], v[32:35]\n\t"
    "ds_read_b64_tr_b16 a[120:121], v17 offset:0\n\t"
    "v_mfma_f32_16x16x32_bf16 v[32:35], a[92:95], a[48:51], v[32:35]\n\t"
    "buffer_load_dwordx4 v19, s[44:47], s61 offen offset:1024 lds\n\t"
    "v_mfma_f32_16x16x32_bf16 v[88:91], a[92:95], a[52:55], v[88:91]\n\t"
    "ds_read_b64_tr_b16 a[122:123], v17 offset:1024\n\t"
    "v_mfma_f32_16x16x32_bf16 v[88:91], a[64:67], a[36:39], v[88:91]\n\t"
    "ds_read_b64_tr_b16 a[148:149], v17 offset:8192\n\t"
    "v_mfma_f32_16x16x32_bf16 v[144:147], a[64:67], a[40:43], v[144:147]\n\t"
    "ds_read_b64_tr_b16 a[150:151], v17 offset:9216\n\t"
    "v_mfma_f32_16x16x32_bf16 v[144:147], a[92:95], a[56:59], v[144:147]\n\t"
    "ds_read_b64_tr_b16 a[124:125], v10 offset:16384\n\t"
    "v_mfma_f32_16x16x32_bf16 v[200:203], a[92:95], a[60:63], v[200:203]\n\t"
    "buffer_load_dwordx4 v20, s[44:47], s62 offen offset:2048 lds\n\t"
    "v_mfma_f32_16x16x32_bf16 v[200:203], a[64:67], a[44:47], v[200:203]\n\t"
    "ds_read_b64_tr_b16 a[126:127], v10 offset:17408\n\t"
    "v_mfma_f32_16x16x32_bf16 v[204:207], a[68:71], a[44:47], v[204:207]\n\t"
    "ds_read_b64_tr_b16 a[152:153], v10 offset:24576\n\t"
    "v_mfma_f32_16x16x32_bf16 v[204:207], a[96:99], a[60:63], v[204:207]\n\t"
    "ds_read_b64_tr_b16 a[154:155], v10 offset:25600\n\t"
    "v_mfma_f32_16x16x32_bf16 v[148:151], a[96:99], a[56:59], v[148:151]\n\t"
    "ds_read_b64_tr_b16 a[128:129], v11 offset:16384\n\t"
    "v_mfma_f32_16x16x32_bf16 v[148:151], a[68:71], a[40:43], v[148:151]\n\t"
    "buffer_load_dwordx4 v20, s[44:47], s63 offen offset:3072 lds\n\t"
    "v_mfma_f32_16x16x32_bf16 v[92:95], a[68:71], a[36:39], v[92:95]\n\t"
    "ds_read_b64_tr_b16 a[130:131], v11 offset:17408\n\t"
    "v_mfma_f32_16x16x32_bf16 v[92:95], a[96:99], a[52:55], v[92:95]\n\t"
    "ds_read_b64_tr_b16 a[156:157], v11 offset:24576\n\t"
    "v_mfma_f32_16x16x32_bf16 v[36:39], a[96:99], a[48:51], v[36:39]\n\t"
    "ds_read_b64_tr_b16 a[158:159], v11 offset:25600\n\t"
    "v_mfma_f32_16x16x32_bf16 v[36:39], a[68:71], a[32:35], v[36:39]\n\t"
    "ds_read_b64_tr_b16 a[132:133], v12 offset:16384\n\t"
    "s_bitset1_b32 m0, 14\n\t"
    "v_mfma_f32_16x16x32_bf16 v[40:43], a[72:75], a[32:35], v[40:43]\n\t"
    "buffer_load_dwordx4 v21, s[44:47], s64 offen offset:0 lds\n\t"
    "v_mfma_f32_16x16x32_bf16 v[40:43], a[100:103], a[48:51], v[40:43]\n\t"
    "ds_read_b64_tr_b16 a[134:135], v12 offset:17408\n\t"
    "v_mfma_f32_16x16x32_bf16 v[96:99], a[100:103], a[52:55], v[96:99]\n\t"
    "ds_read_b64_tr_b16 a[160:161], v12 offset:24576\n\t"
    "v_mfma_f32_16x16x32_bf16 v[96:99], a[72:75], a[36:39], v[96:99]\n\t"
    "ds_read_b64_tr_b16 a[162:163], v12 offset:25600\n\t"
    "v_mfma_f32_16x16x32_bf16 v[152:155], a[72:75], a[40:43], v[152:155]\n\t"
    "ds_read_b64_tr_b16 a[136:137], v13 offset:16384\n\t"
    "v_mfma_f32_16x16x32_bf16 v[152:155], a[100:103], a[56:59], v[152:155]\n\t"
    "buffer_load_dwordx4 v21, s[44:47], s65 offen offset:1024 lds\n\t"
    "v_mfma_f32_16x16x32_bf16 v[208:211], a[100:103], a[60:63], v[208:211]\n\t"
    "ds_read_b64_tr_b16 a[138:139], v13 offset:17408\n\t"
    "v_mfma_f32_16x16x32_bf16 v[208:211], a[72:75], a[44:47], v[208:211]\n\t"
    "ds_read_b64_tr_b16 a[164:165], v13 offset:24576\n\t"
    "v_mfma_f32_16x16x32_bf16 v[212:215], a[76:79], a[44:47], v[212:215]\n\t"
    "ds_read_b64_tr_b16 a[166:167], v13 offset:25600\n\t"
    "v_mfma_f32_16x16x32_bf16 v[212:215], a[104:107], a[60:63], v[212:215]\n\t"
    "ds_read_b64_tr_b16 a[140:141], v14 offset:16384\n\t"
    "v_mfma_f32_16x16x32_bf16 v[156:159], a[104:107], a[56:59], v[156:159]\n\t"
    "buffer_load_dwordx4 v22, s[44:47], s66 offen offset:2048 lds\n\t"
    "v_mfma_f32_16x16x32_bf16 v[156:159], a[76:79], a[40:43], v[156:159]\n\t"
    "ds_read_b64_tr_b16 a[142:143], v14 offset:17408\n\t"
    "v_mfma_f32_16x16x32_bf16 v[100:103], a[76:79], a[36:39], v[100:103]\n\t"
    "ds_read_b64_tr_b16 a[168:169], v14 offset:24576\n\t"
    "v_mfma_f32_16x16x32_bf16 v[100:103], a[104:107], a[52:55], v[100:103]\n\t"
    "ds_read_b64_tr_b16 a[170:171], v14 offset:25600\n\t"
    "v_mfma_f32_16x16x32_bf16 v[44:47], a[104:107], a[48:51], v[44:47]\n\t"
    "ds_read_b64_tr_b16 a[144:145], v15 offset:16384\n\t"
    "v_mfma_f32_16x16x32_bf16 v[44:47], a[76:79], a[32:35], v[44:47]\n\t"
    "buffer_load_dwordx4 v22, s[44:47], s67 offen offset:3072 lds\n\t"
    "v_mfma_f32_16x16x32_bf16 v[48:51], a[80:83], a[32:35], v[48:51]\n\t"
    "ds_read_b64_tr_b16 a[146:147], v15 offset:17408\n\t"
    "buffer_load_dword v28, v7, s[92:95], s81 offen\n\t"
    "v_mfma_f32_16x16x32_bf16 v[48:51], a[108:111], a[48:51], v[48:51]\n\t"
    "ds_read_b64_tr_b16 a[172:173], v15 offset:24576\n\t"
    "s_add_u32 s81, s81, s83\n\t"
    "v_mfma_f32_16x16x32_bf16 v[104:107], a[108:111], a[52:55], v[104:107]\n\t"
    "ds_read_b64_tr_b16 a[174:175], v15 offset:25600\n\t"
    "s_cmp_eq_u32 s81, s85\n\t"
    "v_mfma_f32_16x16x32_bf16 v[104:107], a[80:83], a[36:39], v[104:107]\n\t"
    "s_cbranch_scc1 Lt1%=\n\tLt1b%=:\n\t"
    "s_add_u32 s44, s44, 458752\n\t"
    "v_mfma_f32_16x16x32_bf16 v[160:163], a[80:83], a[40:43], v[160:163]\n\t"
    "s_addc_u32 s45, s45, 0\n\t"
    "s_add_u32 s70, s70, s72\n\t"
    "v_mfma_f32_16x16x32_bf16 v[160:163], a[108:111], a[56:59], v[160:163]\n\t"
    "s_sub_u32 s73, s73, 1\n\t"
    "s_cmp_eq_u32 s73, 0\n\t"
    "v_mfma_f32_16x16x32_bf16 v[216:219], a[108:111], a[60:63], v[216:219]\n\t"
    "s_cselect_b32 s71, s76, s75\n\t"
    "s_cselect_b32 s73, 2, s73\n\t"
    "v_mfma_f32_16x16x32_bf16 v[216:219], a[80:83], a[44:47], v[216:219]\n\t"
    "v_add_u32 v8, s71, v8\n\t"
    "v_add_u32 v9, s71, v9\n\t"
    "v_mfma_f32_16x16x32_bf16 v[220:223], a[84:87], a[44:47], v[220:223]\n\t"
    "s_sub_u32 s74, s74, 1\n\t"
    "s_cmp_eq_u32 s74, 0\n\t"
    "v_mfma_f32_16x16x32_bf16 v[220:223], a[112:115], a[60:63], v[220:223]\n\t"
    "s_cselect_b32 s72, s78, s77\n\t"
    "s_cselect_b32 s74, 3, s74\n\t"
    "v_mfma_f32_16x16x32_bf16 v[164:167], a[112:115], a[56:59], v[164:167]\n\t"
    "v_add_u32 v10, s72, v10\n\t"
    "v_add_u32 v11, s72, v11\n\t"
    "v_mfma_f32_16x16x32_bf16 v[164:167], a[84:87], a[40:43], v[164:167]\n\t"
    "v_add_u32 v12, s72, v12\n\t"
    "v_add_u32 v13, s72, v13\n\t"
    "v_mfma_f32_16x16x32_bf16 v[108:111], a[84:87], a[36:39], v[108:111]\n\t"
    "v_add_u32 v14, s72, v14\n\t"
    "v_add_u32 v15, s72, v15\n\t"
    "v_mfma_f32_16x16x32_bf16 v[108:111], a[112:115], a[52:55], v[108:111]\n\t"
    "v_add_u32 v16, s72, v16\n\t"
    "v_add_u32 v17, s72, v17\n\t"
    "v_mfma_f32_16x16x32_bf16 v[52:55], a[112:115], a[48:51], v[52:55]\n\t"
    "s_sub_u32 s68, s68, 1\n\t"
    "s_cmp_lg_u32 s68, 2\n\t"
    "v_mfma_f32_16x16x32_bf16 v[52:55], a[84:87], a[32:35], v[52:55]\n\t"
    "v_mfma_f32_16x16x32_bf16 v[56:59], a[88:91], a[32:35], v[56:59]\n\t"
    "v_mfma_f32_16x16x32_bf16 v[56:59], a[116:119], a[48:51], v[56:59]\n\t"
    "v_mfma_f32_16x16x32_bf16 v[112:115], a[116:119], a[52:55], v[112:115]\n\t"
    "v_mfma_f32_16x16x32_bf16 v[112:115], a[88:91], a[36:39], v[112:115]\n\t"
    "v_mfma_f32_16x16x32_bf16 v[168:171], a[88:91], a[40:43], v[168:171]\n\t"
    "v_mfma_f32_16x16x32_bf16 v[168:171], a[116:119], a[56:59], v[168:171]\n\t"
    "s_cbranch_scc0 Louth%=\n\t"
    "v_mfma_f32_16x16x32_bf16 v[224:227], a[116:119], a[60:63], v[224:227]\n\t"
    "s_waitcnt vmcnt(8) lgkmcnt(0)\n\t"
    "v_mfma_f32_16x16x32_bf16 v[224:227], a[88:91], a[44:47], v[224:227]\n\t"
    "s_barrier\n\t"
    "v_mfma_f32_16x16x32_bf16 v[60:63], a[120:123], a[32:35], v[60:63]\n\t"
    "ds_read_b64_tr_b16 a[64:65], v10 offset:0\n\t"
    "v_mfma_f32_16x16x32_bf16 v[60:63], a[148:151], a[48:51], v[60:63]\n\t"
    "ds_read_b64_tr_b16 a[66:67], v10 offset:1024\n\t"
    "s_mov_b32 m0, s69\n\t"
    "v_mfma_f32_16x16x32_bf16 v[116:119], a[148:151], a[52:55], v[116:119]\n\t"
    "buffer_load_dwordx4 v18, s[40:43], s52 offen offset:0 sc1 lds\n\t"
    "v_mfma_f32_16x16x32_bf16 v[116:119], a[120:123], a[36:39], v[116:119]\n\t"
    "ds_read_b64_tr_b16 a[92:93], v10 offset:8192\n\t"
    "v_mfma_f32_16x16x32_bf16 v[172:175], a[120:123], a[40:43], v[172:175]\n\t"
    "ds_read_b64_tr_b16 a[94:95], v10 offset:9216\n\t"
    "v_mfma_f32_16x16x32_bf16 v[172:175], a[148:151], a[56:59], v[172:175]\n\t"
    "ds_read_b64_tr_b16 a[68:69], v11 offset:0\n\t"
    "v_mfma_f32_16x16x32_bf16 v[228:231], a[148:151], a[60:63], v[228:231]\n\t"
    "ds_read_b64_tr_b16 a[70:71], v11 offset:1024\n\t"
    "v_mfma_f32_16x16x32_bf16 v[228:231], a[120:123], a[44:47], v[228:231]\n\t"
    "buffer_load_dwordx4 v18, s[40:43], s53 offen offset:1024 sc1 lds\n\t"
    "v_mfma_f32_16x16x32_bf16 v[232:235], a[124:127], a[44:47], v[232:235]\n\t"
    "ds_read_b64_tr_b16 a[96:97], v11 offset:8192\n\t"
    "v_mfma_f32_16x16x32_bf16 v[232:235], a[152:155], a[60:63], v[232:235]\n\t"
    "ds_read_b64_tr_b16 a[98:99], v11 offset:9216\n\t"
    "v_mfma_f32_16x16x32_bf16 v[176:179], a[152:155], a[56:59], v[176:179]\n\t"
    "ds_read_b64_tr_b16 a[72:73], v12 offset:0\n\t"
    "v_mfma_f32_16x16x32_bf16 v[176:179], a[124:127], a[40:43], v[176:179]\n\t"
    "ds_read_b64_tr_b16 a[74:75], v12 offset:1024\n\t"
    "v_mfma_f32_16x16x32_bf16 v[120:123], a[124:127], a[36:39], v[120:123]\n\t"
    "ds_read_b64_tr_b16 a[100:101], v12 offset:8192\n\t"
    "v_mfma_f32_16x16x32_bf16 v[120:123], a[152:155], a[52:55], v[120:123]\n\t"
    "buffer_load_dwordx4 v18, s[40:43], s54 offen offset:2048 sc1 lds\n\t"
    "v_mfma_f32_16x16x32_bf16 v[64:67], a[152:155], a[48:51], v[64:67]\n\t"
    "ds_read_b64_tr_b16 a[102:103], v12 offset:9216\n\t"
    "v_mfma_f32_16x16x32_bf16 v[64:67], a[124:127], a[32:35], v[64:67]\n\t"
    "ds_read_b64_tr_b16 a[76:77], v13 offset:0\n\t"
    "v_mfma_f32_16x16x32_bf16 v[68:71], a[128:131], a[32:35], v[68:71]\n\t"
    "ds_read_b64_tr_b16 a[78:79], v13 offset:1024\n\t"
    "v_mfma_f32_16x16x32_bf16 v[68:71], a[156:159], a[48:51], v[68:71]\n\t"
    "ds_read_b64_tr_b16 a[104:105], v13 offset:8192\n\t"
    "v_mfma_f32_16x16x32_bf16 v[124:127], a[156:159], a[52:55], v[124:127]\n\t"
    "buffer_load_dwordx4 v18, s[40:43], s55 offen offset:3072 sc1 lds\n\t"
    "v_mfma_f32_16x16x32_bf16 v[124:127], a[128:131], a[36:39], v[124:127]\n\t"
    "ds_read_b64_tr_b16 a[106:107], v13 offset:9216\n\t"
    "v_mfma_f32_16x16x32_bf16 v[180:183], a[128:131], a[40:43], v[180:183]\n\t"
    "ds_read_b64_tr_b16 a[80:81], v14 offset:0\n\t"
    "v_mfma_f32_16x16x32_bf16 v[180:183], a[156:159], a[56:59], v[180:183]\n\t"
    "ds_read_b64_tr_b16 a[82:83], v14 offset:1024\n\t"
    "v_mfma_f32_16x16x32_bf16 v[236:239], a[156:159], a[60:63], v[236:239]\n\t"
    "ds_read_b64_tr_b16 a[108:109], v14 offset:8192\n\t"
    "v_mfma_f32_16x16x32_bf16 v[236:239], a[128:131], a[44:47], v[236:239]\n\t"
    "ds_read_b64_tr_b16 a[110:111], v14 offset:9216\n\t"
    "s_bitset1_b32 m0, 14\n\t"
    "v_mfma_f32_16x16x32_bf16 v[240:243], a[132:135], a[44:47], v[240:243]\n\t"
    "buffer_load_dwordx4 v18, s[40:43], s56 offen offset:0 sc1 lds\n\t"
    "v_mfma_f32_16x16x32_bf16 v[240:243], a[160:163], a[60:63], v[240:243]\n\t"
    "ds_read_b64_tr_b16 a[84:85], v15 offset:0\n\t"
    "v_mfma_f32_16x16x32_bf16 v[184:187], a[160:163], a[56:59], v[184:187]\n\t"
    "ds_read_b64_tr_b16 a[86:87], v15 offset:1024\n\t"
    "v_mfma_f32_16x16x32_bf16 v[184:187], a[132:135], a[40:43], v[184:187]\n\t"
    "ds_read_b64_tr_b16 a[112:113], v15 offset:8192\n\t"
    "v_mfma_f32_16x16x32_bf16 v[128:131], a[132:135], a[36:39], v[128:131]\n\t"
    "ds_read_b64_tr_b16 a[114:115], v15 offset:9216\n\t"
    "v_mfma_f32_16x16x32_bf16 v[128:131], a[160:163], a[52:55], v[128:131]\n\t"
    "buffer_load_dwordx4 v18, s[40:43], s57 offen offset:1024 sc1 lds\n\t"
    "v_mfma_f32_16x16x32_bf16 v[72:75], a[160:163], a[48:51], v[72:75]\n\t"
    "ds_read_b64_tr_b16 a[88:89], v16 offset:0\n\t"
    "v_mfma_f32_16x16x32_bf16 v[72:75], a[132:135], a[32:35], v[72:75]\n\t"
    "ds_read_b64_tr_b16 a[90:91], v16 offset:1024\n\t"
    "v_mfma_f32_16x16x32_bf16 v[76:79], a[136:139], a[32:35], v[76:79]\n\t"
    "ds_read_b64_tr_b16 a[116:117], v16 offset:8192\n\t"
    "v_mfma_f32_16x16x32_bf16 v[76:79], a[164:167], a[48:51], v[76:79]\n\t"
    "ds_read_b64_tr_b16 a[118:119], v16 offset:9216\n\t"
    "v_mfma_f32_16x16x32_bf16 v[132:135], a[164:167], a[52:55], v[132:135]\n\t"
    "ds_read_b128 a[0:3], v8 offset:0\n\t"
    "v_mfma_f32_16x16x32_bf16 v[132:135], a[136:139], a[36:39], v[132:135]\n\t"
    "buffer_load_dwordx4 v18, s[40:43], s58 offen offset:2048 sc1 lds\n\t"
    "v_mfma_f32_16x16x32_bf16 v[188:191], a[136:139], a[40:43], v[188:191]\n\t"
    "ds_read_b128 a[4:7], v8 offset:2048\n\t"
    "v_mfma_f32_16x16x32_bf16 v[188:191], a[164:167], a[56:59], v[188:191]\n\t"
    "ds_read_b128 a[8:11], v8 offset:4096\n\t"
    "v_mfma_f32_16x16x32_bf16 v[244:247], a[164:167], a[60:63], v[244:247]\n\t"
    "ds_read_b128 a[12:15], v8 offset:6144\n\t"
    "v_mfma_f32_16x16x32_bf16 v[244:247], a[136:139], a[44:47], v[244:247]\n\t"
    "ds_read_b128 a[16:19], v9 offset:0\n\t"
    "v_mfma_f32_16x16x32_bf16 v[248:251], a[140:143], a[44:47], v[248:251]\n\t"
    "buffer_load_dwordx4 v18, s[40:43], s59 offen offset:3072 sc1 lds\n\t"
    "v_mfma_f32_16x16x32_bf16 v[248:251], a[168:171], a[60:63], v[248:251]\n\t"
    "ds_read_b128 a[20:23], v9 offset:2048\n\t"
    "s_add_u32 s40, s40, 128\n\t"
    "v_mfma_f32_16x16x32_bf16 v[192:195], a[168:171], a[56:59], v[192:195]\n\t"
    "ds_read_b128 a[24:27], v9 offset:4096\n\t"
    "s_addc_u32 s41, s41, 0\n\t"
    "v_mfma_f32_16x16x32_bf16 v[192:195], a[140:143], a[40:43], v[192:195]\n\t"
    "ds_read_b128 a[28:31], v9 offset:6144\n\t"
    "s_add_u32 s69, s69, s71\n\t"
    "v_mfma_f32_16x16x32_bf16 v[136:139], a[140:143], a[36:39], v[136:139]\n\t"
    "v_mfma_f32_16x16x32_bf16 v[136:139], a[168:171], a[52:55], v[136:139]\n\t"
    "v_mfma_f32_16x16x32_bf16 v[80:83], a[168:171], a[48:51], v[80:83]\n\t"
    "v_mfma_f32_16x16x32_bf16 v[80:83], a[140:143], a[32:35], v[80:83]\n\t"
    "v_mfma_f32_16x16x32_bf16 v[84:87], a[144:147], a[32:35], v[84:87]\n\t"
    "v_mfma_f32_16x16x32_bf16 v[84:87], a[172:175], a[48:51], v[84:87]\n\t"
    "v_mfma_f32_16x16x32_bf16 v[140:143], a[172:175], a[52:55], v[140:143]\n\t"
    "v_mfma_f32_16x16x32_bf16 v[140:143], a[144:147], a[36:39], v[140:143]\n\t"
    "s_mov_b32 m0, s70\n\t"
    "v_mfma_f32_16x16x32_bf16 v[196:199], a[144:147], a[40:43], v[196:199]\n\t"
    "buffer_load_dwordx4 v19, s[44:47], s60 offen offset:0 lds\n\t"
    "v_mfma_f32_16x16x32_bf16 v[196:199], a[172:175], a[56:59], v[196:199]\n\t"
    "v_mfma_f32_16x16x32_bf16 v[252:255], a[172:175], a[60:63], v[252:255]\n\t"
    "v_mfma_f32_16x16x32_bf16 v[252:255], a[144:147], a[44:47], v[252:255]\n\t"
    "s_waitcnt lgkmcnt(0)\n\t"
    "v_mfma_f32_16x16x32_bf16 v[32:35], a[64:67], a[0:3], v[32:35]\n\t"
    "ds_read_b64_tr_b16 a[120:121], v17 offset:0\n\t"
    "v_mfma_f32_16x16x32_bf16 v[32:35], a[92:95], a[16:19], v[32:35]\n\t"
    "buffer_load_dwordx4 v19, s[44:47], s61 offen offset:1024 lds\n\t"
    "v_mfma_f32_16x16x32_bf16 v[88:91], a[92:95], a[20:23], v[88:91]\n\t"
    "ds_read_b64_tr_b16 a[122:123], v17 offset:1024\n\t"
    "v_mfma_f32_16x16x32_bf16 v[88:91], a[64:67], a[4:7], v[88:91]\n\t"
    "ds_read_b64_tr_b16 a[148:149], v17 offset:8192\n\t"
    "v_mfma_f32_16x16x32_bf16 v[144:147], a[64:67], a[8:11], v[144:147]\n\t"
    "ds_read_b64_tr_b16 a[150:151], v17 offset:9216\n\t"
    "v_mfma_f32_16x16x32_bf16 v[144:147], a[92:95], a[24:27], v[144:147]\n\t"
    "ds_read_b64_tr_b16 a[124:125], v10 offset:16384\n\t"
    "v_mfma_f32_16x16x32_bf16 v[200:203], a[92:95], a[28:31], v[200:203]\n\t"
    "buffer_load_dwordx4 v20, s[44:47], s62 offen offset:2048 lds\n\t"
    "v_mfma_f32_16x16x32_bf16 v[200:203], a[64:67], a[12:15], v[200:203]\n\t"
    "ds_read_b64_tr_b16 a[126:127], v10 offset:17408\n\t"
    "v_mfma_f32_16x16x32_bf16 v[204:207], a[68:71], a[12:15], v[204:207]\n\t"
    "ds_read_b64_tr_b16 a[152:153], v10 offset:24576\n\t"
    "v_mfma_f32_16x16x32_bf16 v[204:207], a[96:99], a[28:31], v[204:207]\n\t"
    "ds_read_b64_tr_b16 a[154:155], v10 offset:25600\n\t"
    "v_mfma_f32_16x16x32_bf16 v[148:151], a[96:99], a[24:27], v[148:151]\n\t"
    "ds_read_b64_tr_b16 a[128:129], v11 offset:16384\n\t"
    "v_mfma_f32_16x16x32_bf16 v[148:151], a[68:71], a[8:11], v[148:151]\n\t"
    "buffer_load_dwordx4 v20, s[44:47], s63 offen offset:3072 lds\n\t"
    "v_mfma_f32_16x16x32_bf16 v[92:95], a[68:71], a[4:7], v[92:95]\n\t"
    "ds_read_b64_tr_b16 a[130:131], v11 offset:17408\n\t"
    "v_mfma_f32_16x16x32_bf16 v[92:95], a[96:99], a[20:23], v[92:95]\n\t"
    "ds_read_b64_tr_b16 a[156:157], v11 offset:24576\n\t"
    "v_mfma_f32_16x16x32_bf16 v[36:39], a[96:99], a[16:19], v[36:39]\n\t"
    "ds_read_b64_tr_b16 a[158:159], v11 offset:25600\n\t"
    "v_mfma_f32_16x16x32_bf16 v[36:39], a[68:71], a[0:3], v[36:39]\n\t"
    "ds_read_b64_tr_b16 a[132:133], v12 offset:16384\n\t"
    "s_bitset1_b32 m0, 14\n\t"
    "v_mfma_f32_16x16x32_bf16 v[40:43], a[72:75], a[0:3], v[40:43]\n\t"
    "buffer_load_dwordx4 v21, s[44:47], s64 offen offset:0 lds\n\t"
    "v_mfma_f32_16x16x32_bf16 v[40:43], a[100:103], a[16:19], v[40:43]\n\t"
    "ds_read_b64_tr_b16 a[134:135], v12 offset:17408\n\t"
    "v_mfma_f32_16x16x32_bf16 v[96:99], a[100:103], a[20:23], v[96:99]\n\t"
    "ds_read_b64_tr_b16 a[160:161], v12 offset:24576\n\t"
    "v_mfma_f32_16x16x32_bf16 v[96:99], a[72:75], a[4:7], v[96:99]\n\t"
    "ds_read_b64_tr_b16 a[162:163], v12 offset:25600\n\t"
    "v_mfma_f32_16x16x32_bf16 v[152:155], a[72:75], a[8:11], v[152:155]\n\t"
    "ds_read_b64_tr_b16 a[136:137], v13 offset:16384\n\t"
    "v_mfma_f32_16x16x32_bf16 v[152:155], a[100:103], a[24:27], v[152:155]\n\t"
    "buffer_load_dwordx4 v21, s[44:47], s65 offen offset:1024 lds\n\t"
    "v_mfma_f32_16x16x32_bf16 v[208:211], a[100:103], a[28:31], v[208:211]\n\t"
    "ds_read_b64_tr_b16 a[138:139], v13 offset:17408\n\t"
    "v_mfma_f32_16x16x32_bf16 v[208:211], a[72:75], a[12:15], v[208:211]\n\t"
    "ds_read_b64_tr_b16 a[164:165], v13 offset:24576\n\t"
    "v_mfma_f32_16x16x32_bf16 v[212:215], a[76:79], a[12:15], v[212:215]\n\t"
    "ds_read_b64_tr_b16 a[166:167], v13 offset:25600\n\t"
    "v_mfma_f32_16x16x32_bf16 v[212:215], a[104:107], a[28:31], v[212:215]\n\t"
    "ds_read_b64_tr_b16 a[140:141], v14 offset:16384\n\t"
    "v_mfma_f32_16x16x32_bf16 v[156:159], a[104:107], a[24:27], v[156:159]\n\t"
    "buffer_load_dwordx4 v22, s[44:47], s66 offen offset:2048 lds\n\t"
    "v_mfma_f32_16x16x32_bf16 v[156:159], a[76:79], a[8:11], v[156:159]\n\t"
    "ds_read_b64_tr_b16 a[142:143], v14 offset:17408\n\t"
    "v_mfma_f32_16x16x32_bf16 v[100:103], a[76:79], a[4:7], v[100:103]\n\t"
    "ds_read_b64_tr_b16 a[168:169], v14 offset:24576\n\t"
    "v_mfma_f32_16x16x32_bf16 v[100:103], a[104:107], a[20:23], v[100:103]\n\t"
    "ds_read_b64_tr_b16 a[170:171], v14 offset:25600\n\t"
    "v_mfma_f32_16x16x32_bf16 v[44:47], a[104:107], a[16:19], v[44:47]\n\t"
    "ds_read_b64_tr_b16 a[144:145], v15 offset:16384\n\t"
    "v_mfma_f32_16x16x32_bf16 v[44:47], a[76:79], a[0:3], v[44:47]\n\t"
    "buffer_load_dwordx4 v22, s[44:47], s67 offen offset:3072 lds\n\t"
    "v_mfma_f32_16x16x32_bf16 v[48:51], a[80:83], a[0:3], v[48:51]\n\t"
    "ds_read_b64_tr_b16 a[146:147], v15 offset:17408\n\t"
    "buffer_load_dword v28, v7, s[92:95], s81 offen\n\t"
    "v_mfma_f32_16x16x32_bf16 v[48:51], a[108:111], a[16:19], v[48:51]\n\t"
    "ds_read_b64_tr_b16 a[172:173], v15 offset:24576\n\t"
    "s_add_u32 s81, s81, s83\n\t"
    "v_mfma_f32_16x16x32_bf16 v[104:107], a[108:111], a[20:23], v[104:107]\n\t"
    "ds_read_b64_tr_b16 a[174:175], v15 offset:25600\n\t"
    "s_cmp_eq_u32 s81, s85\n\t"
    "v_mfma_f32_16x16x32_bf16 v[104:107], a[80:83], a[4:7], v[104:107]\n\t"
    "s_cbranch_scc1 Lt0%=\n\tLt0b%=:\n\t"
    "s_add_u32 s44, s44, 458752\n\t"
    "v_mfma_f32_16x16x32_bf16 v[160:163], a[80:83], a[8:11], v[160:163]\n\t"
    "s_addc_u32 s45, s45, 0\n\t"
    "s_add_u32 s70, s70, s72\n\t"
    "v_mfma_f32_16x16x32_bf16 v[160:163], a[108:111], a[24:27], v[160:163]\n\t"
    "s_sub_u32 s73, s73, 1\n\t"
    "s_cmp_eq_u32 s73, 0\n\t"
    "v_mfma_f32_16x16x32_bf16 v[216:219], a[108:111], a[28:31], v[216:219]\n\t"
    "s_cselect_b32 s71, s76, s75\n\t"
    "s_cselect_b32 s73, 2, s73\n\t"
    "v_mfma_f32_16x16x32_bf16 v[216:219], a[80:83], a[12:15], v[216:219]\n\t"
    "v_add_u32 v8, s71, v8\n\t"
    "v_add_u32 v9, s71, v9\n\t"
    "v_mfma_f32_16x16x32_bf16 v[220:223], a[84:87], a[12:15], v[220:223]\n\t"
    "s_sub_u32 s74, s74, 1\n\t"
    "s_cmp_eq_u32 s74, 0\n\t"
    "v_mfma_f32_16x16x32_bf16 v[220:223], a[112:115], a[28:31], v[220:223]\n\t"
    "s_cselect_b32 s72, s78, s77\n\t"
    "s_cselect_b32 s74, 3, s74\n\t"
    "v_mfma_f32_16x16x32_bf16 v[164:167], a[112:115], a[24:27], v[164:167]\n\t"
    "v_add_u32 v10, s72, v10\n\t"
    "v_add_u32 v11, s72, v11\n\t"
    "v_mfma_f32_16x16x32_bf16 v[164:167], a[84:87], a[8:11], v[164:167]\n\t"
    "v_add_u32 v12, s72, v12\n\t"
    "v_add_u32 v13, s72, v13\n\t"
    "v_mfma_f32_16x16x32_bf16 v[108:111], a[84:87], a[4:7], v[108:111]\n\t"
    "v_add_u32 v14, s72, v14\n\t"
    "v_add_u32 v15, s72, v15\n\t"
    "v_mfma_f32_16x16x32_bf16 v[108:111], a[112:115], a[20:23], v[108:111]\n\t"
    "v_add_u32 v16, s72, v16\n\t"
    "v_add_u32 v17, s72, v17\n\t"
    "v_mfma_f32_16x16x32_bf16 v[52:55], a[112:115], a[16:19], v[52:55]\n\t"
    "s_sub_u32 s68, s68, 1\n\t"
    "v_mfma_f32_16x16x32_bf16 v[52:55], a[84:87], a[0:3], v[52:55]\n\t"
    "v_mfma_f32_16x16x32_bf16 v[56:59], a[88:91], a[0:3], v[56:59]\n\t"
    "v_mfma_f32_16x16x32_bf16 v[56:59], a[116:119], a[16:19], v[56:59]\n\t"
    "v_mfma_f32_16x16x32_bf16 v[112:115], a[116:119], a[20:23], v[112:115]\n\t"
    "v_mfma_f32_16x16x32_bf16 v[112:115], a[88:91], a[4:7], v[112:115]\n\t"
    "v_mfma_f32_16x16x32_bf16 v[168:171], a[88:91], a[8:11], v[168:171]\n\t"
    "v_mfma_f32_16x16x32_bf16 v[168:171], a[116:119], a[24:27], v[168:171]\n\t"
    "s_branch Llooph%=\n\t"
    "Louth%=:\n\t"
    "v_mfma_f32_16x16x32_bf16 v[224:227], a[116:119], a[60:63], v[224:227]\n\t"
    "s_waitcnt vmcnt(8) lgkmcnt(0)\n\t"
    "v_mfma_f32_16x16x32_bf16 v[224:227], a[88:91], a[44:47], v[224:227]\n\t"
    "s_barrier\n\t"
    "v_mfma_f32_16x16x32_bf16 v[60:63], a[120:123], a[32:35], v[60:63]\n\t"
    "ds_read_b64_tr_b16 a[64:65], v10 offset:0\n\t"
    "s_mov_b32 m0, s69\n\t"
    "v_mfma_f32_16x16x32_bf16 v[60:63], a[148:151], a[48:51], v[60:63]\n\t"
    "buffer_load_dwordx4 v18, s[40:43], s52 offen offset:0 sc1 lds\n\t"
    "v_mfma_f32_16x16x32_bf16 v[116:119], a[148:151], a[52:55], v[116:119]\n\t"
    "ds_read_b64_tr_b16 a[66:67], v10 offset:1024\n\t"
    "v_mfma_f32_16x16x32_bf16 v[116:119], a[120:123], a[36:39], v[116:119]\n\t"
    "buffer_load_dwordx4 v18, s[40:43], s53 offen offset:1024 sc1 lds\n\t"
    "v_mfma_f32_16x16x32_bf16 v[172:175], a[120:123], a[40:43], v[172:175]\n\t"
    "ds_read_b64_tr_b16 a[72:73], v10 offset:8192\n\t"
    "v_mfma_f32_16x16x32_bf16 v[172:175], a[148:151], a[56:59], v[172:175]\n\t"
    "ds_read_b64_tr_b16 a[74:75], v10 offset:9216\n\t"
    "v_mfma_f32_16x16x32_bf16 v[228:231], a[148:151], a[60:63], v[228:231]\n\t"
    "buffer_load_dwordx4 v18, s[40:43], s54 offen offset:2048 sc1 lds\n\t"
    "v_mfma_f32_16x16x32_bf16 v[228:231], a[120:123], a[44:47], v[228:231]\n\t"
    "ds_read_b64_tr_b16 a[68:69], v11 offset:0\n\t"
    "v_mfma_f32_16x16x32_bf16 v[232:235], a[124:127], a[44:47], v[232:235]\n\t"
    "buffer_load_dwordx4 v18, s[40:43], s55 offen offset:3072 sc1 lds\n\t"
    "v_mfma_f32_16x16x32_bf16 v[232:235], a[152:155], a[60:63], v[232:235]\n\t"
    "ds_read_b64_tr_b16 a[70:71], v11 offset:1024\n\t"
    "v_mfma_f32_16x16x32_bf16 v[176:179], a[152:155], a[56:59], v[176:179]\n\t"
    "ds_read_b64_tr_b16 a[76:77], v11 offset:8192\n\t"
    "s_bitset1_b32 m0, 14\n\t"
    "v_mfma_f32_16x16x32_bf16 v[176:179], a[124:127], a[40:43], v[176:179]\n\t"
    "buffer_load_dwordx4 v18, s[40:43], s56 offen offset:0 sc1 lds\n\t"
    "v_mfma_f32_16x16x32_bf16 v[120:123], a[124:127], a[36:39], v[120:123]\n\t"
    "ds_read_b64_tr_b16 a[78:79], v11 offset:9216\n\t"
    "v_mfma_f32_16x16x32_bf16 v[120:123], a[152:155], a[52:55], v[120:123]\n\t"
    "ds_read_b128 a[176:179], v8 offset:0\n\t"
    "v_mfma_f32_16x16x32_bf16 v[64:67], a[152:155], a[48:51], v[64:67]\n\t"
    "buffer_load_dwordx4 v18, s[40:43], s57 offen offset:1024 sc1 lds\n\t"
    "v_mfma_f32_16x16x32_bf16 v[64:67], a[124:127], a[32:35], v[64:67]\n\t"
    "ds_read_b128 a[180:183], v8 offset:2048\n\t"
    "v_mfma_f32_16x16x32_bf16 v[68:71], a[128:131], a[32:35], v[68:71]\n\t"
    "buffer_load_dwordx4 v18, s[40:43], s58 offen offset:2048 sc1 lds\n\t"
    "v_mfma_f32_16x16x32_bf16 v[68:71], a[156:159], a[48:51], v[68:71]\n\t"
    "ds_read_b128 a[184:187], v8 offset:4096\n\t"
    "v_mfma_f32_16x16x32_bf16 v[124:127], a[156:159], a[52:55], v[124:127]\n\t"
    "ds_read_b128 a[188:191], v8 offset:6144\n\t"
    "v_mfma_f32_16x16x32_bf16 v[124:127], a[128:131], a[36:39], v[124:127]\n\t"
    "buffer_load_dwordx4 v18, s[40:43], s59 offen offset:3072 sc1 lds\n\t"
    "v_mfma_f32_16x16x32_bf16 v[180:183], a[128:131], a[40:43], v[180:183]\n\t"
    "ds_read_b128 a[192:195], v9 offset:0\n\t"
    "v_mfma_f32_16x16x32_bf16 v[180:183], a[156:159], a[56:59], v[180:183]\n\t"
    "ds_read_b128 a[196:199], v9 offset:2048\n\t"
    "v_mfma_f32_16x16x32_bf16 v[236:239], a[156:159], a[60:63], v[236:239]\n\t"
    "ds_read_b128 a[200:203], v9 offset:4096\n\t"
    "s_add_u32 s40, s40, 128\n\t"
    "v_mfma_f32_16x16x32_bf16 v[236:239], a[128:131], a[44:47], v[236:239]\n\t"
    "s_addc_u32 s41, s41, 0\n\t"
    "s_add_u32 s69, s69, s71\n\t"
    "v_mfma_f32_16x16x32_bf16 v[240:243], a[132:135], a[44:47], v[240:243]\n\t"
    "v_xor_b32 v24, 32768, v8\n\t"
    "v_xor_b32 v25, 32768, v9\n\t"
    "v_mfma_f32_16x16x32_bf16 v[240:243], a[160:163], a[60:63], v[240:243]\n\t"
    "s_cmp_eq_u32 s74, 1\n\t"
    "s_cselect_b32 s72, s78, s77\n\t"
    "v_mfma_f32_16x16x32_bf16 v[184:187], a[160:163], a[56:59], v[184:187]\n\t"
    "s_bfe_u32 s24, %[b], 0x10001\n\t"
    "s_mul_i32 s31, s24, 3670016\n\t"
    "v_mfma_f32_16x16x32_bf16 v[184:187], a[132:135], a[40:43], v[184:187]\n\t"
    "s_and_b32 s98, s26, 7\n\t"
    "s_lshl_b32 s98, s98, 8\n\t"
    "v_mfma_f32_16x16x32_bf16 v[128:131], a[132:135], a[36:39], v[128:131]\n\t"
    "s_add_u32 s31, s31, s98\n\t"
    "s_add_u32 s38, s16, s31\n\t"
    "v_mfma_f32_16x16x32_bf16 v[128:131], a[160:163], a[52:55], v[128:131]\n\t"
    "s_addc_u32 s39, s17, 0\n\t"
    "s_and_b32 s98, %[b], 1\n\t"
    "v_mfma_f32_16x16x32_bf16 v[72:75], a[160:163], a[48:51], v[72:75]\n\t"
    "s_mul_i32 s87, s98, 3538944\n\t"
    "s_mul_i32 s98, s98, 262144\n\t"
    "v_mfma_f32_16x16x32_bf16 v[72:75], a[132:135], a[32:35], v[72:75]\n\t"
    "s_sub_u32 s80, 131072, s98\n\t"
    "s_add_u32 s44, s38, s87\n\t"
    "v_mfma_f32_16x16x32_bf16 v[76:79], a[136:139], a[32:35], v[76:79]\n\t"
    "s_addc_u32 s45, s39, 0\n\t"
    "s_mov_b32 s46, 0x7fffffff\n\t"
    "v_mfma_f32_16x16x32_bf16 v[76:79], a[164:167], a[48:51], v[76:79]\n\t"
    "s_mov_b32 s47, 0x00020000\n\t"
    "v_mov_b32 v19, v2\n\t"
    "v_mfma_f32_16x16x32_bf16 v[132:135], a[164:167], a[52:55], v[132:135]\n\t"
    "v_xor_b32 v20, 128, v2\n\t"
    "s_add_u32 s70, s23, 98304\n\t"
    "v_mfma_f32_16x16x32_bf16 v[132:135], a[136:139], a[36:39], v[132:135]\n\t"
    "s_mov_b32 s77, 16384\n\t"
    "s_mov_b32 s60, 0\n\t"
    "v_mfma_f32_16x16x32_bf16 v[188:191], a[136:139], a[40:43], v[188:191]\n\t"
    "s_mov_b32 s61, 7168\n\t"
    "s_mov_b32 s62, 14336\n\t"
    "v_mfma_f32_16x16x32_bf16 v[188:191], a[164:167], a[56:59], v[188:191]\n\t"
    "s_mov_b32 s63, 21504\n\t"
    "s_mov_b32 m0, s70\n\t"
    "v_mfma_f32_16x16x32_bf16 v[244:247], a[164:167], a[60:63], v[244:247]\n\t"
    "buffer_load_dwordx4 v19, s[44:47], s60 offen offset:0 lds\n\t"
    "v_mfma_f32_16x16x32_bf16 v[244:247], a[136:139], a[44:47], v[244:247]\n\t"
    "ds_read_b128 a[204:207], v9 offset:6144\n\t"
    "v_mfma_f32_16x16x32_bf16 v[248:251], a[140:143], a[44:47], v[248:251]\n\t"
    "buffer_load_dwordx4 v20, s[44:47], s61 offen offset:1024 lds\n\t"
    "v_mfma_f32_16x16x32_bf16 v[248:251], a[168:171], a[60:63], v[248:251]\n\t"
    "v_mfma_f32_16x16x32_bf16 v[192:195], a[168:171], a[56:59], v[192:195]\n\t"
    "buffer_load_dwordx4 v19, s[44:47], s62 offen offset:2048 lds\n\t"
    "v_mfma_f32_16x16x32_bf16 v[192:195], a[140:143], a[40:43], v[192:195]\n\t"
    "v_mfma_f32_16x16x32_bf16 v[136:139], a[140:143], a[36:39], v[136:139]\n\t"
    "buffer_load_dwordx4 v20, s[44:47], s63 offen offset:3072 lds\n\t"
    "s_mul_i32 s44, s80, 1\n\t"
    "s_add_u32 s44, s44, s87\n\t"
    "s_add_u32 s44, s44, s38\n\t"
    "s_addc_u32 s45, s39, 0\n\t"
    "v_mfma_f32_16x16x32_bf16 v[136:139], a[168:171], a[52:55], v[136:139]\n\t"
    "s_add_u32 s70, s70, s77\n\t"
    "s_mov_b32 m0, s70\n\t"
    "v_mfma_f32_16x16x32_bf16 v[80:83], a[168:171], a[48:51], v[80:83]\n\t"
    "buffer_load_dwordx4 v19, s[44:47], s60 offen offset:0 lds\n\t"
    "v_mfma_f32_16x16x32_bf16 v[80:83], a[140:143], a[32:35], v[80:83]\n\t"
    "v_mfma_f32_16x16x32_bf16 v[84:87], a[144:147], a[32:35], v[84:87]\n\t"
    "buffer_load_dwordx4 v20, s[44:47], s61 offen offset:1024 lds\n\t"
    "v_mfma_f32_16x16x32_bf16 v[84:87], a[172:175], a[48:51], v[84:87]\n\t"
    "v_mfma_f32_16x16x32_bf16 v[140:143], a[172:175], a[52:55], v[140:143]\n\t"
    "buffer_load_dwordx4 v19, s[44:47], s62 offen offset:2048 lds\n\t"
    "v_mfma_f32_16x16x32_bf16 v[140:143], a[144:147], a[36:39], v[140:143]\n\t"
    "v_mfma_f32_16x16x32_bf16 v[196:199], a[144:147], a[40:43], v[196:199]\n\t"
    "buffer_load_dwordx4 v20, s[44:47], s63 offen offset:3072 lds\n\t"
    "v_mfma_f32_16x16x32_bf16 v[196:199], a[172:175], a[56:59], v[196:199]\n\t"
    "s_mul_i32 s44, s80, 5\n\t"
    "s_add_u32 s44, s44, s87\n\t"
    "v_mfma_f32_16x16x32_bf16 v[252:255], a[172:175], a[60:63], v[252:255]\n\t"
    "s_add_u32 s44, s44, s38\n\t"
    "s_addc_u32 s45, s39, 0\n\t"
    "v_mfma_f32_16x16x32_bf16 v[252:255], a[144:147], a[44:47], v[252:255]\n\t"
    "s_add_u32 s70, s70, s77\n\t"
    "s_waitcnt vmcnt(8) lgkmcnt(0)\n\t"
    "s_barrier\n\t"
    "v_mfma_f32_16x16x32_bf16 v[32:35], a[64:67], a[176:179], v[32:35]\n\t"
    "v_add_u32 v26, s72, v10\n\t"
    "ds_read_b64_tr_b16 a[80:81], v26 offset:0\n\t"
    "v_mfma_f32_16x16x32_bf16 v[32:35], a[72:75], a[192:195], v[32:35]\n\t"
    "ds_read_b64_tr_b16 a[82:83], v26 offset:1024\n\t"
    "ds_read_b64_tr_b16 a[88:89], v26 offset:8192\n\t"
    "v_mfma_f32_16x16x32_bf16 v[36:39], a[68:71], a[176:179], v[36:39]\n\t"
    "ds_read_b64_tr_b16 a[90:91], v26 offset:9216\n\t"
    "v_add_u32 v27, s72, v11\n\t"
    "v_mfma_f32_16x16x32_bf16 v[36:39], a[76:79], a[192:195], v[36:39]\n\t"
    "ds_read_b64_tr_b16 a[84:85], v27 offset:0\n\t"
    "ds_read_b64_tr_b16 a[86:87], v27 offset:1024\n\t"
    "v_mfma_f32_16x16x32_bf16 v[92:95], a[68:71], a[180:183], v[92:95]\n\t"
    "ds_read_b64_tr_b16 a[92:93], v27 offset:8192\n\t"
    "ds_read_b64_tr_b16 a[94:95], v27 offset:9216\n\t"
    "v_mfma_f32_16x16x32_bf16 v[92:95], a[76:79], a[196:199], v[92:95]\n\t"
    "ds_read_b128 a[208:211], v24 offset:0\n\t"
    "ds_read_b128 a[212:215], v24 offset:2048\n\t"
    "v_mfma_f32_16x16x32_bf16 v[88:91], a[64:67], a[180:183], v[88:91]\n\t"
    "ds_read_b128 a[216:219], v24 offset:4096\n\t"
    "ds_read_b128 a[220:223], v24 offset:6144\n\t"
    "v_mfma_f32_16x16x32_bf16 v[88:91], a[72:75], a[196:199], v[88:91]\n\t"
    "ds_read_b128 a[224:227], v25 offset:0\n\t"
    "ds_read_b128 a[228:231], v25 offset:2048\n\t"
    "v_mfma_f32_16x16x32_bf16 v[144:147], a[64:67], a[184:187], v[144:147]\n\t"
    "ds_read_b128 a[232:235], v25 offset:4096\n\t"
    "ds_read_b128 a[236:239], v25 offset:6144\n\t"
    "v_mfma_f32_16x16x32_bf16 v[144:147], a[72:75], a[200:203], v[144:147]\n\t"
    "v_mfma_f32_16x16x32_bf16 v[148:151], a[68:71], a[184:187], v[148:151]\n\t"
    "v_mfma_f32_16x16x32_bf16 v[148:151], a[76:79], a[200:203], v[148:151]\n\t"
    "v_mfma_f32_16x16x32_bf16 v[204:207], a[68:71], a[188:191], v[204:207]\n\t"
    "v_mfma_f32_16x16x32_bf16 v[204:207], a[76:79], a[204:207], v[204:207]\n\t"
    "v_mfma_f32_16x16x32_bf16 v[200:203], a[64:67], a[188:191], v[200:203]\n\t"
    "v_mfma_f32_16x16x32_bf16 v[200:203], a[72:75], a[204:207], v[200:203]\n\t"
    "s_waitcnt lgkmcnt(0)\n\t"
    "v_mfma_f32_16x16x32_bf16 v[32:35], a[80:83], a[208:211], v[32:35]\n\t"
    "ds_read_b64_tr_b16 a[0:1], v12 offset:0\n\t"
    "ds_read_b64_tr_b16 a[2:3], v12 offset:1024\n\t"
    "v_mfma_f32_16x16x32_bf16 v[32:35], a[88:91], a[224:227], v[32:35]\n\t"
    "ds_read_b64_tr_b16 a[8:9], v12 offset:8192\n\t"
    "ds_read_b64_tr_b16 a[10:11], v12 offset:9216\n\t"
    "v_mfma_f32_16x16x32_bf16 v[36:39], a[84:87], a[208:211], v[36:39]\n\t"
    "ds_read_b64_tr_b16 a[4:5], v13 offset:0\n\t"
    "ds_read_b64_tr_b16 a[6:7], v13 offset:1024\n\t"
    "v_mfma_f32_16x16x32_bf16 v[36:39], a[92:95], a[224:227], v[36:39]\n\t"
    "ds_read_b64_tr_b16 a[12:13], v13 offset:8192\n\t"
    "ds_read_b64_tr_b16 a[14:15], v13 offset:9216\n\t"
    "v_mfma_f32_16x16x32_bf16 v[92:95], a[84:87], a[212:215], v[92:95]\n\t"
    "v_add_u32 v26, s72, v12\n\t"
    "ds_read_b64_tr_b16 a[16:17], v26 offset:0\n\t"
    "v_mfma_f32_16x16x32_bf16 v[92:95], a[92:95], a[228:231], v[92:95]\n\t"
    "ds_read_b64_tr_b16 a[18:19], v26 offset:1024\n\t"
    "ds_read_b64_tr_b16 a[24:25], v26 offset:8192\n\t"
    "v_mfma_f32_16x16x32_bf16 v[88:91], a[80:83], a[212:215], v[88:91]\n\t"
    "ds_read_b64_tr_b16 a[26:27], v26 offset:9216\n\t"
    "v_add_u32 v27, s72, v13\n\t"
    "v_mfma_f32_16x16x32_bf16 v[88:91], a[88:91], a[228:231], v[88:91]\n\t"
    "ds_read_b64_tr_b16 a[20:21], v27 offset:0\n\t"
    "ds_read_b64_tr_b16 a[22:23], v27 offset:1024\n\t"
    "v_mfma_f32_16x16x32_bf16 v[144:147], a[80:83], a[216:219], v[144:147]\n\t"
    "ds_read_b64_tr_b16 a[28:29], v27 offset:8192\n\t"
    "ds_read_b64_tr_b16 a[30:31], v27 offset:9216\n\t"
    "v_mfma_f32_16x16x32_bf16 v[144:147], a[88:91], a[232:235], v[144:147]\n\t"
    "v_mfma_f32_16x16x32_bf16 v[148:151], a[84:87], a[216:219], v[148:151]\n\t"
    "v_mfma_f32_16x16x32_bf16 v[148:151], a[92:95], a[232:235], v[148:151]\n\t"
    "v_mfma_f32_16x16x32_bf16 v[204:207], a[84:87], a[220:223], v[204:207]\n\t"
    "v_mfma_f32_16x16x32_bf16 v[204:207], a[92:95], a[236:239], v[204:207]\n\t"
    "v_mfma_f32_16x16x32_bf16 v[200:203], a[80:83], a[220:223], v[200:203]\n\t"
    "v_mfma_f32_16x16x32_bf16 v[200:203], a[88:91], a[236:239], v[200:203]\n\t"
    "s_waitcnt lgkmcnt(0)\n\t"
    "v_mfma_f32_16x16x32_bf16 v[40:43], a[0:3], a[176:179], v[40:43]\n\t"
    "ds_read_b64_tr_b16 a[64:65], v14 offset:0\n\t"
    "s_add_u32 s82, s35, 0\n\t"
    "v_mfma_f32_16x16x32_bf16 v[40:43], a[8:11], a[192:195], v[40:43]\n\t"
    "ds_read_b64_tr_b16 a[66:67], v14 offset:1024\n\t"
    "v_cvt_pk_bf16_f32 v32, v32, v33\n\t"
    "v_mfma_f32_16x16x32_bf16 v[40:43], a[16:19], a[208:211], v[40:43]\n\t"
    "ds_read_b64_tr_b16 a[72:73], v14 offset:8192\n\t"
    "v_cvt_pk_bf16_f32 v33, v34, v35\n\t"
    "v_mfma_f32_16x16x32_bf16 v[40:43], a[24:27], a[224:227], v[40:43]\n\t"
    "ds_read_b64_tr_b16 a[74:75], v14 offset:9216\n\t"
    "v_cvt_pk_bf16_f32 v34, v36, v37\n\t"
    "v_mfma_f32_16x16x32_bf16 v[44:47], a[4:7], a[176:179], v[44:47]\n\t"
    "ds_read_b64_tr_b16 a[68:69], v15 offset:0\n\t"
    "v_cvt_pk_bf16_f32 v35, v38, v39\n\t"
    "v_mfma_f32_16x16x32_bf16 v[44:47], a[12:15], a[192:195], v[44:47]\n\t"
    "ds_read_b64_tr_b16 a[70:71], v15 offset:1024\n\t"
    "v_pk_max_i16 v32, v32, 0\n\t"
    "v_mfma_f32_16x16x32_bf16 v[44:47], a[20:23], a[208:211], v[44:47]\n\t"
    "ds_read_b64_tr_b16 a[76:77], v15 offset:8192\n\t"
    "v_pk_max_i16 v33, v33, 0\n\t"
    "v_mfma_f32_16x16x32_bf16 v[44:47], a[28:31], a[224:227], v[44:47]\n\t"
    "ds_read_b64_tr_b16 a[78:79], v15 offset:9216\n\t"
    "v_pk_max_i16 v34, v34, 0\n\t"
    "v_mfma_f32_16x16x32_bf16 v[100:103], a[4:7], a[180:183], v[100:103]\n\t"
    "v_add_u32 v26, s72, v14\n\t"
    "v_pk_max_i16 v35, v35, 0\n\t"
    "v_mfma_f32_16x16x32_bf16 v[100:103], a[12:15], a[196:199], v[100:103]\n\t"
    "ds_read_b64_tr_b16 a[80:81], v26 offset:0\n\t"
    "buffer_store_dwordx4 v[32:35], v23, s[48:51], s82 offen offset:0 sc1\n\t"
    "v_mfma_f32_16x16x32_bf16 v[100:103], a[20:23], a[212:215], v[100:103]\n\t"
    "ds_read_b64_tr_b16 a[82:83], v26 offset:1024\n\t"
    "s_add_u32 s82, s35, 0\n\t"
    "v_mfma_f32_16x16x32_bf16 v[100:103], a[28:31], a[228:231], v[100:103]\n\t"
    "ds_read_b64_tr_b16 a[88:89], v26 offset:8192\n\t"
    "v_cvt_pk_bf16_f32 v88, v88, v89\n\t"
    "v_mfma_f32_16x16x32_bf16 v[96:99], a[0:3], a[180:183], v[96:99]\n\t"
    "ds_read_b64_tr_b16 a[90:91], v26 offset:9216\n\t"
    "v_cvt_pk_bf16_f32 v89, v90, v91\n\t"
    "v_mfma_f32_16x16x32_bf16 v[96:99], a[8:11], a[196:199], v[96:99]\n\t"
    "v_add_u32 v27, s72, v15\n\t"
    "v_cvt_pk_bf16_f32 v90, v92, v93\n\t"
    "v_mfma_f32_16x16x32_bf16 v[96:99], a[16:19], a[212:215], v[96:99]\n\t"
    "ds_read_b64_tr_b16 a[84:85], v27 offset:0\n\t"
    "v_cvt_pk_bf16_f32 v91, v94, v95\n\t"
    "v_mfma_f32_16x16x32_bf16 v[96:99], a[24:27], a[228:231], v[96:99]\n\t"
    "ds_read_b64_tr_b16 a[86:87], v27 offset:1024\n\t"
    "v_pk_max_i16 v88, v88, 0\n\t"
    "v_mfma_f32_16x16x32_bf16 v[152:155], a[0:3], a[184:187], v[152:155]\n\t"
    "ds_read_b64_tr_b16 a[92:93], v27 offset:8192\n\t"
    "v_pk_max_i16 v89, v89, 0\n\t"
    "v_mfma_f32_16x16x32_bf16 v[152:155], a[8:11], a[200:203], v[152:155]\n\t"
    "ds_read_b64_tr_b16 a[94:95], v27 offset:9216\n\t"
    "v_pk_max_i16 v90, v90, 0\n\t"
    "v_mfma_f32_16x16x32_bf16 v[152:155], a[16:19], a[216:219], v[152:155]\n\t"
    "v_pk_max_i16 v91, v91, 0\n\t"
    "buffer_store_dwordx4 v[88:91], v23, s[48:51], s82 offen offset:1024 sc1\n\t"
    "v_mfma_f32_16x16x32_bf16 v[152:155], a[24:27], a[232:235], v[152:155]\n\t"
    "s_add_u32 s82, s35, 0\n\t"
    "v_cvt_pk_bf16_f32 v144, v144, v145\n\t"
    "v_mfma_f32_16x16x32_bf16 v[156:159], a[4:7], a[184:187], v[156:159]\n\t"
    "v_cvt_pk_bf16_f32 v145, v146, v147\n\t"
    "v_cvt_pk_bf16_f32 v146, v148, v149\n\t"
    "v_mfma_f32_16x16x32_bf16 v[156:159], a[12:15], a[200:203], v[156:159]\n\t"
    "v_cvt_pk_bf16_f32 v147, v150, v151\n\t"
    "v_pk_max_i16 v144, v144, 0\n\t"
    "v_mfma_f32_16x16x32_bf16 v[156:159], a[20:23], a[216:219], v[156:159]\n\t"
    "v_pk_max_i16 v145, v145, 0\n\t"
    "v_pk_max_i16 v146, v146, 0\n\t"
    "v_mfma_f32_16x16x32_bf16 v[156:159], a[28:31], a[232:235], v[156:159]\n\t"
    "v_pk_max_i16 v147, v147, 0\n\t"
    "buffer_store_dwordx4 v[144:147], v23, s[48:51], s82 offen offset:2048 sc1\n\t"
    "v_mfma_f32_16x16x32_bf16 v[212:215], a[4:7], a[188:191], v[212:215]\n\t"
    "s_add_u32 s82, s35, 0\n\t"
    "v_cvt_pk_bf16_f32 v200, v200, v201\n\t"
    "v_mfma_f32_16x16x32_bf16 v[212:215], a[12:15], a[204:207], v[212:215]\n\t"
    "v_cvt_pk_bf16_f32 v201, v202, v203\n\t"
    "v_cvt_pk_bf16_f32 v202, v204, v205\n\t"
    "v_mfma_f32_16x16x32_bf16 v[212:215], a[20:23], a[220:223], v[212:215]\n\t"
    "v_cvt_pk_bf16_f32 v203, v206, v207\n\t"
    "v_pk_max_i16 v200, v200, 0\n\t"
    "v_mfma_f32_16x16x32_bf16 v[212:215], a[28:31], a[236:239], v[212:215]\n\t"
    "v_pk_max_i16 v201, v201, 0\n\t"
    "v_pk_max_i16 v202, v202, 0\n\t"
    "v_mfma_f32_16x16x32_bf16 v[208:211], a[0:3], a[188:191], v[208:211]\n\t"
    "v_pk_max_i16 v203, v203, 0\n\t"
    "buffer_store_dwordx4 v[200:203], v23, s[48:51], s82 offen offset:3072 sc1\n\t"
    "v_mfma_f32_16x16x32_bf16 v[208:211], a[8:11], a[204:207], v[208:211]\n\t"
    "v_mfma_f32_16x16x32_bf16 v[208:211], a[16:19], a[220:223], v[208:211]\n\t"
    "v_mfma_f32_16x16x32_bf16 v[208:211], a[24:27], a[236:239], v[208:211]\n\t"
    "s_waitcnt lgkmcnt(0)\n\t"
    "v_mfma_f32_16x16x32_bf16 v[48:51], a[64:67], a[176:179], v[48:51]\n\t"
    "ds_read_b64_tr_b16 a[0:1], v16 offset:0\n\t"
    "s_add_u32 s82, s35, 131072\n\t"
    "v_mfma_f32_16x16x32_bf16 v[48:51], a[72:75], a[192:195], v[48:51]\n\t"
    "ds_read_b64_tr_b16 a[2:3], v16 offset:1024\n\t"
    "v_cvt_pk_bf16_f32 v40, v40, v41\n\t"
    "v_mfma_f32_16x16x32_bf16 v[48:51], a[80:83], a[208:211], v[48:51]\n\t"
    "ds_read_b64_tr_b16 a[8:9], v16 offset:8192\n\t"
    "v_cvt_pk_bf16_f32 v41, v42, v43\n\t"
    "v_mfma_f32_16x16x32_bf16 v[48:51], a[88:91], a[224:227], v[48:51]\n\t"
    "ds_read_b64_tr_b16 a[10:11], v16 offset:9216\n\t"
    "v_cvt_pk_bf16_f32 v42, v44, v45\n\t"
    "v_mfma_f32_16x16x32_bf16 v[52:55], a[68:71], a[176:179], v[52:55]\n\t"
    "ds_read_b64_tr_b16 a[4:5], v17 offset:0\n\t"
    "v_cvt_pk_bf16_f32 v43, v46, v47\n\t"
    "v_mfma_f32_16x16x32_bf16 v[52:55], a[76:79], a[192:195], v[52:55]\n\t"
    "ds_read_b64_tr_b16 a[6:7], v17 offset:1024\n\t"
    "v_pk_max_i16 v40, v40, 0\n\t"
    "v_mfma_f32_16x16x32_bf16 v[52:55], a[84:87], a[208:211], v[52:55]\n\t"
    "ds_read_b64_tr_b16 a[12:13], v17 offset:8192\n\t"
    "v_pk_max_i16 v41, v41, 0\n\t"
    "v_mfma_f32_16x16x32_bf16 v[52:55], a[92:95], a[224:227], v[52:55]\n\t"
    "ds_read_b64_tr_b16 a[14:15], v17 offset:9216\n\t"
    "v_pk_max_i16 v42, v42, 0\n\t"
    "v_mfma_f32_16x16x32_bf16 v[108:111], a[68:71], a[180:183], v[108:111]\n\t"
    "v_add_u32 v26, s72, v16\n\t"
    "v_pk_max_i16 v43, v43, 0\n\t"
    "v_mfma_f32_16x16x32_bf16 v[108:111], a[76:79], a[196:199], v[108:111]\n\t"
    "ds_read_b64_tr_b16 a[16:17], v26 offset:0\n\t"
    "buffer_store_dwordx4 v[40:43], v23, s[48:51], s82 offen offset:0 sc1\n\t"
    "v_mfma_f32_16x16x32_bf16 v[108:111], a[84:87], a[212:215], v[108:111]\n\t"
    "ds_read_b64_tr_b16 a[18:19], v26 offset:1024\n\t"
    "s_add_u32 s82, s35, 131072\n\t"
    "v_mfma_f32_16x16x32_bf16 v[108:111], a[92:95], a[228:231], v[108:111]\n\t"
    "ds_read_b64_tr_b16 a[24:25], v26 offset:8192\n\t"
    "v_cvt_pk_bf16_f32 v96, v96, v97\n\t"
    "v_mfma_f32_16x16x32_bf16 v[104:107], a[64:67], a[180:183], v[104:107]\n\t"
    "ds_read_b64_tr_b16 a[26:27], v26 offset:9216\n\t"
    "v_cvt_pk_bf16_f32 v97, v98, v99\n\t"
    "v_mfma_f32_16x16x32_bf16 v[104:107], a[72:75], a[196:199], v[104:107]\n\t"
    "v_add_u32 v27, s72, v17\n\t"
    "v_cvt_pk_bf16_f32 v98, v100, v101\n\t"
    "v_mfma_f32_16x16x32_bf16 v[104:107], a[80:83], a[212:215], v[104:107]\n\t"
    "ds_read_b64_tr_b16 a[20:21], v27 offset:0\n\t"
    "v_cvt_pk_bf16_f32 v99, v102, v103\n\t"
    "v_mfma_f32_16x16x32_bf16 v[104:107], a[88:91], a[228:231], v[104:107]\n\t"
    "ds_read_b64_tr_b16 a[22:23], v27 offset:1024\n\t"
    "v_pk_max_i16 v96, v96, 0\n\t"
    "v_mfma_f32_16x16x32_bf16 v[160:163], a[64:67], a[184:187], v[160:163]\n\t"
    "ds_read_b64_tr_b16 a[28:29], v27 offset:8192\n\t"
    "v_pk_max_i16 v97, v97, 0\n\t"
    "v_mfma_f32_16x16x32_bf16 v[160:163], a[72:75], a[200:203], v[160:163]\n\t"
    "ds_read_b64_tr_b16 a[30:31], v27 offset:9216\n\t"
    "v_pk_max_i16 v98, v98, 0\n\t"
    "v_mfma_f32_16x16x32_bf16 v[160:163], a[80:83], a[216:219], v[160:163]\n\t"
    "v_pk_max_i16 v99, v99, 0\n\t"
    "buffer_store_dwordx4 v[96:99], v23, s[48:51], s82 offen offset:1024 sc1\n\t"
    "v_mfma_f32_16x16x32_bf16 v[160:163], a[88:91], a[232:235], v[160:163]\n\t"
    "s_add_u32 s82, s35, 131072\n\t"
    "v_cvt_pk_bf16_f32 v152, v152, v153\n\t"
    "v_mfma_f32_16x16x32_bf16 v[164:167], a[68:71], a[184:187], v[164:167]\n\t"
    "v_cvt_pk_bf16_f32 v153, v154, v155\n\t"
    "v_cvt_pk_bf16_f32 v154, v156, v157\n\t"
    "v_mfma_f32_16x16x32_bf16 v[164:167], a[76:79], a[200:203], v[164:167]\n\t"
    "v_cvt_pk_bf16_f32 v155, v158, v159\n\t"
    "v_pk_max_i16 v152, v152, 0\n\t"
    "v_mfma_f32_16x16x32_bf16 v[164:167], a[84:87], a[216:219], v[164:167]\n\t"
    "v_pk_max_i16 v153, v153, 0\n\t"
    "v_pk_max_i16 v154, v154, 0\n\t"
    "v_mfma_f32_16x16x32_bf16 v[164:167], a[92:95], a[232:235], v[164:167]\n\t"
    "v_pk_max_i16 v155, v155, 0\n\t"
    "buffer_store_dwordx4 v[152:155], v23, s[48:51], s82 offen offset:2048 sc1\n\t"
    "v_mfma_f32_16x16x32_bf16 v[220:223], a[68:71], a[188:191], v[220:223]\n\t"
    "s_add_u32 s82, s35, 131072\n\t"
    "v_cvt_pk_bf16_f32 v208, v208, v209\n\t"
    "v_mfma_f32_16x16x32_bf16 v[220:223], a[76:79], a[204:207], v[220:223]\n\t"
    "v_cvt_pk_bf16_f32 v209, v210, v211\n\t"
    "v_cvt_pk_bf16_f32 v210, v212, v213\n\t"
    "v_mfma_f32_16x16x32_bf16 v[220:223], a[84:87], a[220:223], v[220:223]\n\t"
    "v_cvt_pk_bf16_f32 v211, v214, v215\n\t"
    "v_pk_max_i16 v208, v208, 0\n\t"
    "v_mfma_f32_16x16x32_bf16 v[220:223], a[92:95], a[236:239], v[220:223]\n\t"
    "v_pk_max_i16 v209, v209, 0\n\t"
    "v_pk_max_i16 v210, v210, 0\n\t"
    "v_mfma_f32_16x16x32_bf16 v[216:219], a[64:67], a[188:191], v[216:219]\n\t"
    "v_pk_max_i16 v211, v211, 0\n\t"
    "buffer_store_dwordx4 v[208:211], v23, s[48:51], s82 offen offset:3072 sc1\n\t"
    "v_mfma_f32_16x16x32_bf16 v[216:219], a[72:75], a[204:207], v[216:219]\n\t"
    "v_mfma_f32_16x16x32_bf16 v[216:219], a[80:83], a[220:223], v[216:219]\n\t"
    "v_mfma_f32_16x16x32_bf16 v[216:219], a[88:91], a[236:239], v[216:219]\n\t"
    "s_waitcnt lgkmcnt(0)\n\t"
    "v_mfma_f32_16x16x32_bf16 v[56:59], a[0:3], a[176:179], v[56:59]\n\t"
    "ds_read_b64_tr_b16 a[64:65], v10 offset:16384\n\t"
    "s_add_u32 s82, s35, 262144\n\t"
    "v_mfma_f32_16x16x32_bf16 v[56:59], a[8:11], a[192:195], v[56:59]\n\t"
    "ds_read_b64_tr_b16 a[66:67], v10 offset:17408\n\t"
    "v_cvt_pk_bf16_f32 v48, v48, v49\n\t"
    "v_mfma_f32_16x16x32_bf16 v[56:59], a[16:19], a[208:211], v[56:59]\n\t"
    "ds_read_b64_tr_b16 a[72:73], v10 offset:24576\n\t"
    "v_cvt_pk_bf16_f32 v49, v50, v51\n\t"
    "v_mfma_f32_16x16x32_bf16 v[56:59], a[24:27], a[224:227], v[56:59]\n\t"
    "ds_read_b64_tr_b16 a[74:75], v10 offset:25600\n\t"
    "v_cvt_pk_bf16_f32 v50, v52, v53\n\t"
    "v_mfma_f32_16x16x32_bf16 v[60:63], a[4:7], a[176:179], v[60:63]\n\t"
    "ds_read_b64_tr_b16 a[68:69], v11 offset:16384\n\t"
    "v_cvt_pk_bf16_f32 v51, v54, v55\n\t"
    "v_mfma_f32_16x16x32_bf16 v[60:63], a[12:15], a[192:195], v[60:63]\n\t"
    "ds_read_b64_tr_b16 a[70:71], v11 offset:17408\n\t"
    "v_pk_max_i16 v48, v48, 0\n\t"
    "v_mfma_f32_16x16x32_bf16 v[60:63], a[20:23], a[208:211], v[60:63]\n\t"
    "ds_read_b64_tr_b16 a[76:77], v11 offset:24576\n\t"
    "v_pk_max_i16 v49, v49, 0\n\t"
    "v_mfma_f32_16x16x32_bf16 v[60:63], a[28:31], a[224:227], v[60:63]\n\t"
    "ds_read_b64_tr_b16 a[78:79], v11 offset:25600\n\t"
    "v_pk_max_i16 v50, v50, 0\n\t"
    "v_mfma_f32_16x16x32_bf16 v[116:119], a[4:7], a[180:183], v[116:119]\n\t"
    "v_add_u32 v26, s72, v10\n\t"
    "v_pk_max_i16 v51, v51, 0\n\t"
    "v_mfma_f32_16x16x32_bf16 v[116:119], a[12:15], a[196:199], v[116:119]\n\t"
    "ds_read_b64_tr_b16 a[80:81], v26 offset:16384\n\t"
    "buffer_store_dwordx4 v[48:51], v23, s[48:51], s82 offen offset:0 sc1\n\t"
    "v_mfma_f32_16x16x32_bf16 v[116:119], a[20:23], a[212:215], v[116:119]\n\t"
    "ds_read_b64_tr_b16 a[82:83], v26 offset:17408\n\t"
    "s_add_u32 s82, s35, 262144\n\t"
    "v_mfma_f32_16x16x32_bf16 v[116:119], a[28:31], a[228:231], v[116:119]\n\t"
    "ds_read_b64_tr_b16 a[88:89], v26 offset:24576\n\t"
    "v_cvt_pk_bf16_f32 v104, v104, v105\n\t"
    "v_mfma_f32_16x16x32_bf16 v[112:115], a[0:3], a[180:183], v[112:115]\n\t"
    "ds_read_b64_tr_b16 a[90:91], v26 offset:25600\n\t"
    "v_cvt_pk_bf16_f32 v105, v106, v107\n\t"
    "v_mfma_f32_16x16x32_bf16 v[112:115], a[8:11], a[196:199], v[112:115]\n\t"
    "v_add_u32 v27, s72, v11\n\t"
    "v_cvt_pk_bf16_f32 v106, v108, v109\n\t"
    "v_mfma_f32_16x16x32_bf16 v[112:115], a[16:19], a[212:215], v[112:115]\n\t"
    "ds_read_b64_tr_b16 a[84:85], v27 offset:16384\n\t"
    "v_cvt_pk_bf16_f32 v107, v110, v111\n\t"
    "v_mfma_f32_16x16x32_bf16 v[112:115], a[24:27], a[228:231], v[112:115]\n\t"
    "ds_read_b64_tr_b16 a[86:87], v27 offset:17408\n\t"
    "v_pk_max_i16 v104, v104, 0\n\t"
    "v_mfma_f32_16x16x32_bf16 v[168:171], a[0:3], a[184:187], v[168:171]\n\t"
    "ds_read_b64_tr_b16 a[92:93], v27 offset:24576\n\t"
    "v_pk_max_i16 v105, v105, 0\n\t"
    "v_mfma_f32_16x16x32_bf16 v[168:171], a[8:11], a[200:203], v[168:171]\n\t"
    "ds_read_b64_tr_b16 a[94:95], v27 offset:25600\n\t"
    "v_pk_max_i16 v106, v106, 0\n\t"
    "v_mfma_f32_16x16x32_bf16 v[168:171], a[16:19], a[216:219], v[168:171]\n\t"
    "v_pk_max_i16 v107, v107, 0\n\t"
    "buffer_store_dwordx4 v[104:107], v23, s[48:51], s82 offen offset:1024 sc1\n\t"
    "v_mfma_f32_16x16x32_bf16 v[168:171], a[24:27], a[232:235], v[168:171]\n\t"
    "s_add_u32 s82, s35, 262144\n\t"
    "v_cvt_pk_bf16_f32 v160, v160, v161\n\t"
    "v_mfma_f32_16x16x32_bf16 v[172:175], a[4:7], a[184:187], v[172:175]\n\t"
    "v_cvt_pk_bf16_f32 v161, v162, v163\n\t"
    "v_cvt_pk_bf16_f32 v162, v164, v165\n\t"
    "v_mfma_f32_16x16x32_bf16 v[172:175], a[12:15], a[200:203], v[172:175]\n\t"
    "v_cvt_pk_bf16_f32 v163, v166, v167\n\t"
    "v_pk_max_i16 v160, v160, 0\n\t"
    "v_mfma_f32_16x16x32_bf16 v[172:175], a[20:23], a[216:219], v[172:175]\n\t"
    "v_pk_max_i16 v161, v161, 0\n\t"
    "v_pk_max_i16 v162, v162, 0\n\t"
    "v_mfma_f32_16x16x32_bf16 v[172:175], a[28:31], a[232:235], v[172:175]\n\t"
    "v_pk_max_i16 v163, v163, 0\n\t"
    "buffer_store_dwordx4 v[160:163], v23, s[48:51], s82 offen offset:2048 sc1\n\t"
    "v_mfma_f32_16x16x32_bf16 v[228:231], a[4:7], a[188:191], v[228:231]\n\t"
    "s_add_u32 s82, s35, 262144\n\t"
    "v_cvt_pk_bf16_f32 v216, v216, v217\n\t"
    "v_mfma_f32_16x16x32_bf16 v[228:231], a[12:15], a[204:207], v[228:231]\n\t"
    "v_cvt_pk_bf16_f32 v217, v218, v219\n\t"
    "v_cvt_pk_bf16_f32 v218, v220, v221\n\t"
    "v_mfma_f32_16x16x32_bf16 v[228:231], a[20:23], a[220:223], v[228:231]\n\t"
    "v_cvt_pk_bf16_f32 v219, v222, v223\n\t"
    "v_pk_max_i16 v216, v216, 0\n\t"
    "v_mfma_f32_16x16x32_bf16 v[228:231], a[28:31], a[236:239], v[228:231]\n\t"
    "v_pk_max_i16 v217, v217, 0\n\t"
    "v_pk_max_i16 v218, v218, 0\n\t"
    "v_mfma_f32_16x16x32_bf16 v[224:227], a[0:3], a[188:191], v[224:227]\n\t"
    "v_pk_max_i16 v219, v219, 0\n\t"
    "buffer_store_dwordx4 v[216:219], v23, s[48:51], s82 offen offset:3072 sc1\n\t"
    "v_mfma_f32_16x16x32_bf16 v[224:227], a[8:11], a[204:207], v[224:227]\n\t"
    "v_mfma_f32_16x16x32_bf16 v[224:227], a[16:19], a[220:223], v[224:227]\n\t"
    "v_mfma_f32_16x16x32_bf16 v[224:227], a[24:27], a[236:239], v[224:227]\n\t"
    "s_waitcnt lgkmcnt(0)\n\t"
    "v_mfma_f32_16x16x32_bf16 v[64:67], a[64:67], a[176:179], v[64:67]\n\t"
    "ds_read_b64_tr_b16 a[0:1], v12 offset:16384\n\t"
    "s_add_u32 s82, s35, 393216\n\t"
    "v_mfma_f32_16x16x32_bf16 v[64:67], a[72:75], a[192:195], v[64:67]\n\t"
    "ds_read_b64_tr_b16 a[2:3], v12 offset:17408\n\t"
    "v_cvt_pk_bf16_f32 v56, v56, v57\n\t"
    "v_mfma_f32_16x16x32_bf16 v[64:67], a[80:83], a[208:211], v[64:67]\n\t"
    "ds_read_b64_tr_b16 a[8:9], v12 offset:24576\n\t"
    "v_cvt_pk_bf16_f32 v57, v58, v59\n\t"
    "v_mfma_f32_16x16x32_bf16 v[64:67], a[88:91], a[224:227], v[64:67]\n\t"
    "ds_read_b64_tr_b16 a[10:11], v12 offset:25600\n\t"
    "v_cvt_pk_bf16_f32 v58, v60, v61\n\t"
    "v_mfma_f32_16x16x32_bf16 v[68:71], a[68:71], a[176:179], v[68:71]\n\t"
    "ds_read_b64_tr_b16 a[4:5], v13 offset:16384\n\t"
    "v_cvt_pk_bf16_f32 v59, v62, v63\n\t"
    "v_mfma_f32_16x16x32_bf16 v[68:71], a[76:79], a[192:195], v[68:71]\n\t"
    "ds_read_b64_tr_b16 a[6:7], v13 offset:17408\n\t"
    "v_pk_max_i16 v56, v56, 0\n\t"
    "v_mfma_f32_16x16x32_bf16 v[68:71], a[84:87], a[208:211], v[68:71]\n\t"
    "ds_read_b64_tr_b16 a[12:13], v13 offset:24576\n\t"
    "v_pk_max_i16 v57, v57, 0\n\t"
    "v_mfma_f32_16x16x32_bf16 v[68:71], a[92:95], a[224:227], v[68:71]\n\t"
    "ds_read_b64_tr_b16 a[14:15], v13 offset:25600\n\t"
    "v_pk_max_i16 v58, v58, 0\n\t"
    "v_mfma_f32_16x16x32_bf16 v[124:127], a[68:71], a[180:183], v[124:127]\n\t"
    "v_add_u32 v26, s72, v12\n\t"
    "v_pk_max_i16 v59, v59, 0\n\t"
    "v_mfma_f32_16x16x32_bf16 v[124:127], a[76:79], a[196:199], v[124:127]\n\t"
    "ds_read_b64_tr_b16 a[16:17], v26 offset:16384\n\t"
    "buffer_store_dwordx4 v[56:59], v23, s[48:51], s82 offen offset:0 sc1\n\t"
    "v_mfma_f32_16x16x32_bf16 v[124:127], a[84:87], a[212:215], v[124:127]\n\t"
    "ds_read_b64_tr_b16 a[18:19], v26 offset:17408\n\t"
    "s_add_u32 s82, s35, 393216\n\t"
    "v_mfma_f32_16x16x32_bf16 v[124:127], a[92:95], a[228:231], v[124:127]\n\t"
    "ds_read_b64_tr_b16 a[24:25], v26 offset:24576\n\t"
    "v_cvt_pk_bf16_f32 v112, v112, v113\n\t"
    "v_mfma_f32_16x16x32_bf16 v[120:123], a[64:67], a[180:183], v[120:123]\n\t"
    "ds_read_b64_tr_b16 a[26:27], v26 offset:25600\n\t"
    "v_cvt_pk_bf16_f32 v113, v114, v115\n\t"
    "v_mfma_f32_16x16x32_bf16 v[120:123], a[72:75], a[196:199], v[120:123]\n\t"
    "v_add_u32 v27, s72, v13\n\t"
    "v_cvt_pk_bf16_f32 v114, v116, v117\n\t"
    "v_mfma_f32_16x16x32_bf16 v[120:123], a[80:83], a[212:215], v[120:123]\n\t"
    "ds_read_b64_tr_b16 a[20:21], v27 offset:16384\n\t"
    "v_cvt_pk_bf16_f32 v115, v118, v119\n\t"
    "v_mfma_f32_16x16x32_bf16 v[120:123], a[88:91], a[228:231], v[120:123]\n\t"
    "ds_read_b64_tr_b16 a[22:23], v27 offset:17408\n\t"
    "v_pk_max_i16 v112, v112, 0\n\t"
    "v_mfma_f32_16x16x32_bf16 v[176:179], a[64:67], a[184:187], v[176:179]\n\t"
    "ds_read_b64_tr_b16 a[28:29], v27 offset:24576\n\t"
    "v_pk_max_i16 v113, v113, 0\n\t"
    "v_mfma_f32_16x16x32_bf16 v[176:179], a[72:75], a[200:203], v[176:179]\n\t"
    "ds_read_b64_tr_b16 a[30:31], v27 offset:25600\n\t"
    "v_pk_max_i16 v114, v114, 0\n\t"
    "v_mfma_f32_16x16x32_bf16 v[176:179], a[80:83], a[216:219], v[176:179]\n\t"
    "v_pk_max_i16 v115, v115, 0\n\t"
    "buffer_store_dwordx4 v[112:115], v23, s[48:51], s82 offen offset:1024 sc1\n\t"
    "v_mfma_f32_16x16x32_bf16 v[176:179], a[88:91], a[232:235], v[176:179]\n\t"
    "s_add_u32 s82, s35, 393216\n\t"
    "v_cvt_pk_bf16_f32 v168, v168, v169\n\t"
    "v_mfma_f32_16x16x32_bf16 v[180:183], a[68:71], a[184:187], v[180:183]\n\t"
    "v_cvt_pk_bf16_f32 v169, v170, v171\n\t"
    "v_cvt_pk_bf16_f32 v170, v172, v173\n\t"
    "v_mfma_f32_16x16x32_bf16 v[180:183], a[76:79], a[200:203], v[180:183]\n\t"
    "v_cvt_pk_bf16_f32 v171, v174, v175\n\t"
    "v_pk_max_i16 v168, v168, 0\n\t"
    "v_mfma_f32_16x16x32_bf16 v[180:183], a[84:87], a[216:219], v[180:183]\n\t"
    "v_pk_max_i16 v169, v169, 0\n\t"
    "v_pk_max_i16 v170, v170, 0\n\t"
    "v_mfma_f32_16x16x32_bf16 v[180:183], a[92:95], a[232:235], v[180:183]\n\t"
    "v_pk_max_i16 v171, v171, 0\n\t"
    "buffer_store_dwordx4 v[168:171], v23, s[48:51], s82 offen offset:2048 sc1\n\t"
    "v_mfma_f32_16x16x32_bf16 v[236:239], a[68:71], a[188:191], v[236:239]\n\t"
    "s_add_u32 s82, s35, 393216\n\t"
    "v_cvt_pk_bf16_f32 v224, v224, v225\n\t"
    "v_mfma_f32_16x16x32_bf16 v[236:239], a[76:79], a[204:207], v[236:239]\n\t"
    "v_cvt_pk_bf16_f32 v225, v226, v227\n\t"
    "v_cvt_pk_bf16_f32 v226, v228, v229\n\t"
    "v_mfma_f32_16x16x32_bf16 v[236:239], a[84:87], a[220:223], v[236:239]\n\t"
    "v_cvt_pk_bf16_f32 v227, v230, v231\n\t"
    "v_pk_max_i16 v224, v224, 0\n\t"
    "v_mfma_f32_16x16x32_bf16 v[236:239], a[92:95], a[236:239], v[236:239]\n\t"
    "v_pk_max_i16 v225, v225, 0\n\t"
    "v_pk_max_i16 v226, v226, 0\n\t"
    "v_mfma_f32_16x16x32_bf16 v[232:235], a[64:67], a[188:191], v[232:235]\n\t"
    "v_pk_max_i16 v227, v227, 0\n\t"
    "buffer_store_dwordx4 v[224:227], v23, s[48:51], s82 offen offset:3072 sc1\n\t"
    "v_mfma_f32_16x16x32_bf16 v[232:235], a[72:75], a[204:207], v[232:235]\n\t"
    "v_mfma_f32_16x16x32_bf16 v[232:235], a[80:83], a[220:223], v[232:235]\n\t"
    "v_mfma_f32_16x16x32_bf16 v[232:235], a[88:91], a[236:239], v[232:235]\n\t"
    "s_waitcnt lgkmcnt(0)\n\t"
    "v_mfma_f32_16x16x32_bf16 v[72:75], a[0:3], a[176:179], v[72:75]\n\t"
    "ds_read_b64_tr_b16 a[64:65], v14 offset:16384\n\t"
    "s_add_u32 s82, s79, 524288\n\t"
    "v_mfma_f32_16x16x32_bf16 v[72:75], a[8:11], a[192:195], v[72:75]\n\t"
    "ds_read_b64_tr_b16 a[66:67], v14 offset:17408\n\t"
    "v_cvt_pk_bf16_f32 v64, v64, v65\n\t"
    "v_mfma_f32_16x16x32_bf16 v[72:75], a[16:19], a[208:211], v[72:75]\n\t"
    "ds_read_b64_tr_b16 a[72:73], v14 offset:24576\n\t"
    "v_cvt_pk_bf16_f32 v65, v66, v67\n\t"
    "v_mfma_f32_16x16x32_bf16 v[72:75], a[24:27], a[224:227], v[72:75]\n\t"
    "ds_read_b64_tr_b16 a[74:75], v14 offset:25600\n\t"
    "v_cvt_pk_bf16_f32 v66, v68, v69\n\t"
    "v_mfma_f32_16x16x32_bf16 v[76:79], a[4:7], a[176:179], v[76:79]\n\t"
    "ds_read_b64_tr_b16 a[68:69], v15 offset:16384\n\t"
    "v_cvt_pk_bf16_f32 v67, v70, v71\n\t"
    "v_mfma_f32_16x16x32_bf16 v[76:79], a[12:15], a[192:195], v[76:79]\n\t"
    "ds_read_b64_tr_b16 a[70:71], v15 offset:17408\n\t"
    "v_pk_max_i16 v64, v64, 0\n\t"
    "v_mfma_f32_16x16x32_bf16 v[76:79], a[20:23], a[208:211], v[76:79]\n\t"
    "ds_read_b64_tr_b16 a[76:77], v15 offset:24576\n\t"
    "v_pk_max_i16 v65, v65, 0\n\t"
    "v_mfma_f32_16x16x32_bf16 v[76:79], a[28:31], a[224:227], v[76:79]\n\t"
    "ds_read_b64_tr_b16 a[78:79], v15 offset:25600\n\t"
    "v_pk_max_i16 v66, v66, 0\n\t"
    "v_mfma_f32_16x16x32_bf16 v[132:135], a[4:7], a[180:183], v[132:135]\n\t"
    "v_add_u32 v26, s72, v14\n\t"
    "v_pk_max_i16 v67, v67, 0\n\t"
    "v_mfma_f32_16x16x32_bf16 v[132:135], a[12:15], a[196:199], v[132:135]\n\t"
    "ds_read_b64_tr_b16 a[80:81], v26 offset:16384\n\t"
    "buffer_store_dwordx4 v[64:67], v23, s[48:51], s82 offen offset:0 sc1\n\t"
    "v_mfma_f32_16x16x32_bf16 v[132:135], a[20:23], a[212:215], v[132:135]\n\t"
    "ds_read_b64_tr_b16 a[82:83], v26 offset:17408\n\t"
    "s_add_u32 s82, s79, 524288\n\t"
    "v_mfma_f32_16x16x32_bf16 v[132:135], a[28:31], a[228:231], v[132:135]\n\t"
    "ds_read_b64_tr_b16 a[88:89], v26 offset:24576\n\t"
    "v_cvt_pk_bf16_f32 v120, v120, v121\n\t"
    "v_mfma_f32_16x16x32_bf16 v[128:131], a[0:3], a[180:183], v[128:131]\n\t"
    "ds_read_b64_tr_b16 a[90:91], v26 offset:25600\n\t"
    "v_cvt_pk_bf16_f32 v121, v122, v123\n\t"
    "v_mfma_f32_16x16x32_bf16 v[128:131], a[8:11], a[196:199], v[128:131]\n\t"
    "v_add_u32 v27, s72, v15\n\t"
    "v_cvt_pk_bf16_f32 v122, v124, v125\n\t"
    "v_mfma_f32_16x16x32_bf16 v[128:131], a[16:19], a[212:215], v[128:131]\n\t"
    "ds_read_b64_tr_b16 a[84:85], v27 offset:16384\n\t"
    "v_cvt_pk_bf16_f32 v123, v126, v127\n\t"
    "v_mfma_f32_16x16x32_bf16 v[128:131], a[24:27], a[228:231], v[128:131]\n\t"
    "ds_read_b64_tr_b16 a[86:87], v27 offset:17408\n\t"
    "v_pk_max_i16 v120, v120, 0\n\t"
    "v_mfma_f32_16x16x32_bf16 v[184:187], a[0:3], a[184:187], v[184:187]\n\t"
    "ds_read_b64_tr_b16 a[92:93], v27 offset:24576\n\t"
    "v_pk_max_i16 v121, v121, 0\n\t"
    "v_mfma_f32_16x16x32_bf16 v[184:187], a[8:11], a[200:203], v[184:187]\n\t"
    "ds_read_b64_tr_b16 a[94:95], v27 offset:25600\n\t"
    "v_pk_max_i16 v122, v122, 0\n\t"
    "v_mfma_f32_16x16x32_bf16 v[184:187], a[16:19], a[216:219], v[184:187]\n\t"
    "v_pk_max_i16 v123, v123, 0\n\t"
    "buffer_store_dwordx4 v[120:123], v23, s[48:51], s82 offen offset:1024 sc1\n\t"
    "v_mfma_f32_16x16x32_bf16 v[184:187], a[24:27], a[232:235], v[184:187]\n\t"
    "s_add_u32 s82, s79, 524288\n\t"
    "v_cvt_pk_bf16_f32 v176, v176, v177\n\t"
    "v_mfma_f32_16x16x32_bf16 v[188:191], a[4:7], a[184:187], v[188:191]\n\t"
    "v_cvt_pk_bf16_f32 v177, v178, v179\n\t"
    "v_cvt_pk_bf16_f32 v178, v180, v181\n\t"
    "v_mfma_f32_16x16x32_bf16 v[188:191], a[12:15], a[200:203], v[188:191]\n\t"
    "v_cvt_pk_bf16_f32 v179, v182, v183\n\t"
    "v_pk_max_i16 v176, v176, 0\n\t"
    "v_mfma_f32_16x16x32_bf16 v[188:191], a[20:23], a[216:219], v[188:191]\n\t"
    "v_pk_max_i16 v177, v177, 0\n\t"
    "v_pk_max_i16 v178, v178, 0\n\t"
    "v_mfma_f32_16x16x32_bf16 v[188:191], a[28:31], a[232:235], v[188:191]\n\t"
    "v_pk_max_i16 v179, v179, 0\n\t"
    "buffer_store_dwordx4 v[176:179], v23, s[48:51], s82 offen offset:2048 sc1\n\t"
    "v_mfma_f32_16x16x32_bf16 v[244:247], a[4:7], a[188:191], v[244:247]\n\t"
    "s_add_u32 s82, s79, 524288\n\t"
    "v_cvt_pk_bf16_f32 v232, v232, v233\n\t"
    "v_mfma_f32_16x16x32_bf16 v[244:247], a[12:15], a[204:207], v[244:247]\n\t"
    "v_cvt_pk_bf16_f32 v233, v234, v235\n\t"
    "v_cvt_pk_bf16_f32 v234, v236, v237\n\t"
    "v_mfma_f32_16x16x32_bf16 v[244:247], a[20:23], a[220:223], v[244:247]\n\t"
    "v_cvt_pk_bf16_f32 v235, v238, v239\n\t"
    "v_pk_max_i16 v232, v232, 0\n\t"
    "v_mfma_f32_16x16x32_bf16 v[244:247], a[28:31], a[236:239], v[244:247]\n\t"
    "v_pk_max_i16 v233, v233, 0\n\t"
    "v_pk_max_i16 v234, v234, 0\n\t"
    "v_mfma_f32_16x16x32_bf16 v[240:243], a[0:3], a[188:191], v[240:243]\n\t"
    "v_pk_max_i16 v235, v235, 0\n\t"
    "buffer_store_dwordx4 v[232:235], v23, s[48:51], s82 offen offset:3072 sc1\n\t"
    "v_mfma_f32_16x16x32_bf16 v[240:243], a[8:11], a[204:207], v[240:243]\n\t"
    "s_add_u32 s31, s30, 64\n\t"
    "v_mov_b32 v3, s31\n\t"
    "v_mfma_f32_16x16x32_bf16 v[240:243], a[16:19], a[220:223], v[240:243]\n\t"
    "v_mov_b32 v4, s27\n\t"
    "v_mfma_f32_16x16x32_bf16 v[240:243], a[24:27], a[236:239], v[240:243]\n\t"
    "s_waitcnt lgkmcnt(0)\n\t"
    "s_waitcnt vmcnt(4)\n\t"
    "s_barrier\n\t"
    "v_mfma_f32_16x16x32_bf16 v[80:83], a[64:67], a[176:179], v[80:83]\n\t"
    "s_cmp_eq_u32 s22, 0\n\t"
    "s_cbranch_scc1 Lea%=\n\tLeab%=:\n\t"
    "v_mfma_f32_16x16x32_bf16 v[80:83], a[72:75], a[192:195], v[80:83]\n\t"
    "s_add_u32 s82, s79, 655360\n\t"
    "v_cvt_pk_bf16_f32 v72, v72, v73\n\t"
    "v_mfma_f32_16x16x32_bf16 v[80:83], a[80:83], a[208:211], v[80:83]\n\t"
    "v_cvt_pk_bf16_f32 v73, v74, v75\n\t"
    "v_cvt_pk_bf16_f32 v74, v76, v77\n\t"
    "v_mfma_f32_16x16x32_bf16 v[80:83], a[88:91], a[224:227], v[80:83]\n\t"
    "v_cvt_pk_bf16_f32 v75, v78, v79\n\t"
    "v_pk_max_i16 v72, v72, 0\n\t"
    "v_mfma_f32_16x16x32_bf16 v[84:87], a[68:71], a[176:179], v[84:87]\n\t"
    "v_pk_max_i16 v73, v73, 0\n\t"
    "v_pk_max_i16 v74, v74, 0\n\t"
    "v_mfma_f32_16x16x32_bf16 v[84:87], a[76:79], a[192:195], v[84:87]\n\t"
    "v_pk_max_i16 v75, v75, 0\n\t"
    "buffer_store_dwordx4 v[72:75], v23, s[48:51], s82 offen offset:0 sc1\n\t"
    "v_mfma_f32_16x16x32_bf16 v[84:87], a[84:87], a[208:211], v[84:87]\n\t"
    "s_add_u32 s82, s79, 655360\n\t"
    "v_cvt_pk_bf16_f32 v128, v128, v129\n\t"
    "v_mfma_f32_16x16x32_bf16 v[84:87], a[92:95], a[224:227], v[84:87]\n\t"
    "v_cvt_pk_bf16_f32 v129, v130, v131\n\t"
    "v_cvt_pk_bf16_f32 v130, v132, v133\n\t"
    "v_mfma_f32_16x16x32_bf16 v[140:143], a[68:71], a[180:183], v[140:143]\n\t"
    "v_cvt_pk_bf16_f32 v131, v134, v135\n\t"
    "v_pk_max_i16 v128, v128, 0\n\t"
    "v_mfma_f32_16x16x32_bf16 v[140:143], a[76:79], a[196:199], v[140:143]\n\t"
    "v_pk_max_i16 v129, v129, 0\n\t"
    "v_pk_max_i16 v130, v130, 0\n\t"
    "v_mfma_f32_16x16x32_bf16 v[140:143], a[84:87], a[212:215], v[140:143]\n\t"
    "v_pk_max_i16 v131, v131, 0\n\t"
    "buffer_store_dwordx4 v[128:131], v23, s[48:51], s82 offen offset:1024 sc1\n\t"
    "v_mfma_f32_16x16x32_bf16 v[140:143], a[92:95], a[228:231], v[140:143]\n\t"
    "s_add_u32 s82, s79, 655360\n\t"
    "v_cvt_pk_bf16_f32 v184, v184, v185\n\t"
    "v_mfma_f32_16x16x32_bf16 v[136:139], a[64:67], a[180:183], v[136:139]\n\t"
    "v_cvt_pk_bf16_f32 v185, v186, v187\n\t"
    "v_cvt_pk_bf16_f32 v186, v188, v189\n\t"
    "v_mfma_f32_16x16x32_bf16 v[136:139], a[72:75], a[196:199], v[136:139]\n\t"
    "v_cvt_pk_bf16_f32 v187, v190, v191\n\t"
    "v_pk_max_i16 v184, v184, 0\n\t"
    "v_mfma_f32_16x16x32_bf16 v[136:139], a[80:83], a[212:215], v[136:139]\n\t"
    "v_pk_max_i16 v185, v185, 0\n\t"
    "v_pk_max_i16 v186, v186, 0\n\t"
    "v_mfma_f32_16x16x32_bf16 v[136:139], a[88:91], a[228:231], v[136:139]\n\t"
    "v_pk_max_i16 v187, v187, 0\n\t"
    "buffer_store_dwordx4 v[184:187], v23, s[48:51], s82 offen offset:2048 sc1\n\t"
    "v_mfma_f32_16x16x32_bf16 v[192:195], a[64:67], a[184:187], v[192:195]\n\t"
    "s_add_u32 s82, s79, 655360\n\t"
    "v_cvt_pk_bf16_f32 v240, v240, v241\n\t"
    "v_mfma_f32_16x16x32_bf16 v[192:195], a[72:75], a[200:203], v[192:195]\n\t"
    "v_cvt_pk_bf16_f32 v241, v242, v243\n\t"
    "v_cvt_pk_bf16_f32 v242, v244, v245\n\t"
    "v_mfma_f32_16x16x32_bf16 v[192:195], a[80:83], a[216:219], v[192:195]\n\t"
    "v_cvt_pk_bf16_f32 v243, v246, v247\n\t"
    "v_pk_max_i16 v240, v240, 0\n\t"
    "v_mfma_f32_16x16x32_bf16 v[192:195], a[88:91], a[232:235], v[192:195]\n\t"
    "v_pk_max_i16 v241, v241, 0\n\t"
    "v_pk_max_i16 v242, v242, 0\n\t"
    "v_mfma_f32_16x16x32_bf16 v[196:199], a[68:71], a[184:187], v[196:199]\n\t"
    "v_pk_max_i16 v243, v243, 0\n\t"
    "buffer_store_dwordx4 v[240:243], v23, s[48:51], s82 offen offset:3072 sc1\n\t"
    "v_mfma_f32_16x16x32_bf16 v[196:199], a[76:79], a[200:203], v[196:199]\n\t"
    "v_mfma_f32_16x16x32_bf16 v[196:199], a[84:87], a[216:219], v[196:199]\n\t"
    "v_mfma_f32_16x16x32_bf16 v[196:199], a[92:95], a[232:235], v[196:199]\n\t"
    "v_mfma_f32_16x16x32_bf16 v[252:255], a[68:71], a[188:191], v[252:255]\n\t"
    "v_mfma_f32_16x16x32_bf16 v[252:255], a[76:79], a[204:207], v[252:255]\n\t"
    "v_mfma_f32_16x16x32_bf16 v[252:255], a[84:87], a[220:223], v[252:255]\n\t"
    "v_mfma_f32_16x16x32_bf16 v[252:255], a[92:95], a[236:239], v[252:255]\n\t"
    "v_mfma_f32_16x16x32_bf16 v[248:251], a[64:67], a[188:191], v[248:251]\n\t"
    "v_mfma_f32_16x16x32_bf16 v[248:251], a[72:75], a[204:207], v[248:251]\n\t"
    "v_mfma_f32_16x16x32_bf16 v[248:251], a[80:83], a[220:223], v[248:251]\n\t"
    "v_mfma_f32_16x16x32_bf16 v[248:251], a[88:91], a[236:239], v[248:251]\n\t"
    "s_add_u32 s82, s79, 786432\n\t"
    "v_cvt_pk_bf16_f32 v80, v80, v81\n\t"
    "v_cvt_pk_bf16_f32 v81, v82, v83\n\t"
    "v_cvt_pk_bf16_f32 v82, v84, v85\n\t"
    "v_cvt_pk_bf16_f32 v83, v86, v87\n\t"
    "v_pk_max_i16 v80, v80, 0\n\t"
    "v_pk_max_i16 v81, v81, 0\n\t"
    "v_pk_max_i16 v82, v82, 0\n\t"
    "v_pk_max_i16 v83, v83, 0\n\t"
    "buffer_store_dwordx4 v[80:83], v23, s[48:51], s82 offen offset:0 sc1\n\t"
    "s_add_u32 s82, s79, 786432\n\t"
    "v_cvt_pk_bf16_f32 v136, v136, v137\n\t"
    "v_cvt_pk_bf16_f32 v137, v138, v139\n\t"
    "v_cvt_pk_bf16_f32 v138, v140, v141\n\t"
    "v_cvt_pk_bf16_f32 v139, v142, v143\n\t"
    "v_pk_max_i16 v136, v136, 0\n\t"
    "v_pk_max_i16 v137, v137, 0\n\t"
    "v_pk_max_i16 v138, v138, 0\n\t"
    "v_pk_max_i16 v139, v139, 0\n\t"
    "buffer_store_dwordx4 v[136:139], v23, s[48:51], s82 offen offset:1024 sc1\n\t"
    "s_add_u32 s82, s79, 786432\n\t"
    "v_cvt_pk_bf16_f32 v192, v192, v193\n\t"
    "v_cvt_pk_bf16_f32 v193, v194, v195\n\t"
    "v_cvt_pk_bf16_f32 v194, v196, v197\n\t"
    "v_cvt_pk_bf16_f32 v195, v198, v199\n\t"
    "v_pk_max_i16 v192, v192, 0\n\t"
    "v_pk_max_i16 v193, v193, 0\n\t"
    "v_pk_max_i16 v194, v194, 0\n\t"
    "v_pk_max_i16 v195, v195, 0\n\t"
    "buffer_store_dwordx4 v[192:195], v23, s[48:51], s82 offen offset:2048 sc1\n\t"
    "s_add_u32 s82, s79, 786432\n\t"
    "v_cvt_pk_bf16_f32 v248, v248, v249\n\t"
    "v_cvt_pk_bf16_f32 v249, v250, v251\n\t"
    "v_cvt_pk_bf16_f32 v250, v252, v253\n\t"
    "v_cvt_pk_bf16_f32 v251, v254, v255\n\t"
    "v_pk_max_i16 v248, v248, 0\n\t"
    "v_pk_max_i16 v249, v249, 0\n\t"
    "v_pk_max_i16 v250, v250, 0\n\t"
    "v_pk_max_i16 v251, v251, 0\n\t"
    "buffer_store_dwordx4 v[248:251], v23, s[48:51], s82 offen offset:3072 sc1\n\t"
    "s_branch Ls2%=\n\t"
    "Lp%=:\n\t"
    "s_getpc_b64 s[96:97]\n\t"
    "Lpc%=:\n\t"
    "s_add_u32 s96, s96, k_all-Lpc%=\n\t"
    "s_addc_u32 s97, s97, -1\n\t"
    "s_bfe_u32 s31, %[b], 0x40003\n\t"
    "s_lshl_b32 s31, s31, 12\n\t"
    "v_lshrrev_b32 v3, 3, v0\n\t"
    "v_lshl_add_u32 v3, v3, 7, s31\n\t"
    "v_min_u32 v3, Lend%=-k_all-4, v3\n\t"
    "global_load_dword v28, v3, s[96:97]\n\t"
    "v_lshrrev_b32 v3, 6, v0\n\t"
    "v_and_b32 v1, 63, v0\n\t"
    "v_readfirstlane_b32 s22, v3\n\t"
    "s_and_b32 s24, %[b], 1\n\t"
    "s_bfe_u32 s25, %[b], 0x20001\n\t"
    "s_lshr_b32 s26, %[b], 3\n\t"
    "s_lshl_b32 s23, s22, 12\n\t"
    "s_bfe_u32 s33, s26, 0x10003\n\t"
    "s_and_b32 s34, s26, 7\n\t"
    "s_and_b32 s30, %[b], 7\n\t"
    "s_lshl_b32 s30, s30, 1\n\t"
    "s_add_u32 s30, s30, s33\n\t"
    "s_lshl_b32 s30, s30, 8\n\t"
    "s_lshl_b32 s31, s34, 2\n\t"
    "s_add_u32 s30, s30, s31\n\t"
    "s_add_u32 s30, s30, 16384\n\t"
    "s_lshl_b32 s31, s25, 1\n\t"
    "s_add_u32 s33, s33, s31\n\t"
    "s_mul_i32 s87, s34, 1\n\t"
    "v_lshrrev_b32 v3, 3, v1\n\t"
    "v_and_b32 v4, 6, v3\n\t"
    "v_and_b32 v5, 7, v0\n\t"
    "v_xor_b32 v4, v4, v5\n\t"
    "v_lshl_add_u32 v3, s22, 5, v3\n\t"
    "v_lshlrev_b32 v3, 12, v3\n\t"
    "v_lshl_add_u32 v18, v4, 4, v3\n\t"
    "s_mov_b32 s42, 0x7fffffff\n\t"
    "s_mov_b32 s43, 0x00020000\n\t"
    "s_add_u32 s69, s23, 0\n\t"
    "s_mov_b32 s75, 32768\n\t"
    "s_mov_b32 s52, 0\n\t"
    "s_mov_b32 s53, 31744\n\t"
    "s_mov_b32 s54, 63488\n\t"
    "s_mov_b32 s55, 95232\n\t"
    "s_mov_b32 s56, 524288\n\t"
    "s_mov_b32 s57, 556032\n\t"
    "s_mov_b32 s58, 587776\n\t"
    "s_mov_b32 s59, 619520\n\t"
    "s_lshl_b32 s31, s33, 20\n\t"
    "s_cmp_eq_u32 s24, 0\n\t"
    "s_waitcnt lgkmcnt(0)\n\t"
    "s_cselect_b64 s[36:37], s[6:7], s[8:9]\n\t"
    "s_add_u32 s36, s36, s31\n\t"
    "s_addc_u32 s37, s37, 0\n\t"
    "s_mul_i32 s85, s87, 128\n\t"
    "s_add_u32 s40, s36, s85\n\t"
    "s_addc_u32 s41, s37, 0\n\t"
    "s_mov_b32 s85, 0\n\t"
    "s_lshl_b32 s31, s26, 26\n\t"
    "v_lshlrev_b32 v3, 20, v1\n\t"
    "s_mov_b32 s91, 0x00020000\n\t"
    "s_mov_b64 s[88:89], s[18:19]\n\t"
    "s_mov_b32 s90, 46268416\n\t"
    "s_cmp_eq_u32 s22, 1\n\t"
    "s_cselect_b64 s[88:89], s[20:21], s[88:89]\n\t"
    "s_cselect_b32 s90, 4194304, s90\n\t"
    "s_cmp_eq_u32 s22, 2\n\t"
    "s_cselect_b64 s[88:89], s[16:17], s[88:89]\n\t"
    "s_cselect_b32 s90, 7340032, s90\n\t"
    "s_bitcmp1_b32 %[b], 1\n\t"
    "s_cselect_b64 s[92:93], s[12:13], s[10:11]\n\t"
    "s_cmp_eq_u32 s22, 3\n\t"
    "s_cselect_b64 s[88:89], s[92:93], s[88:89]\n\t"
    "s_cselect_b32 s90, 2097152, s90\n\t"
    "buffer_load_dword v29, v3, s[88:91], s31 offen sc1\n\t"
    "s_sleep 10\n\t"
    "s_mov_b64 s[88:89], s[14:15]\n\t"
    "v_lshlrev_b32 v3, 7, v0\n\t"
    "s_mul_i32 s82, %[b], 32768\n\t"
    "s_add_u32 s82, s82, 0\n\t"
    "s_cmp_ge_u32 s22, 4\n\t"
    "s_cselect_b32 s90, 0, 7340032\n\t"
    "buffer_load_dword v24, v3, s[88:91], s82 offen\n\t"
    "s_mul_i32 s82, %[b], 8192\n\t"
    "s_add_u32 s82, s82, 4194304\n\t"
    "s_cmp_ge_u32 s22, 1\n\t"
    "s_cselect_b32 s90, 0, 7340032\n\t"
    "buffer_load_dword v27, v3, s[88:91], s82 offen\n\t"
    "s_mov_b32 m0, s69\n\t"
    "s_nop 0\n\t"
    "buffer_load_dwordx4 v18, s[40:43], s52 offen offset:0 lds\n\t"
    "buffer_load_dwordx4 v18, s[40:43], s53 offen offset:1024 lds\n\t"
    "buffer_load_dwordx4 v18, s[40:43], s54 offen offset:2048 lds\n\t"
    "buffer_load_dwordx4 v18, s[40:43], s55 offen offset:3072 lds\n\t"
    "s_bitset1_b32 m0, 14\n\t"
    "s_nop 0\n\t"
    "buffer_load_dwordx4 v18, s[40:43], s56 offen offset:0 lds\n\t"
    "buffer_load_dwordx4 v18, s[40:43], s57 offen offset:1024 lds\n\t"
    "buffer_load_dwordx4 v18, s[40:43], s58 offen offset:2048 lds\n\t"
    "buffer_load_dwordx4 v18, s[40:43], s59 offen offset:3072 lds\n\t"
    "s_add_u32 s85, s85, 1\n\t"
    "s_add_u32 s40, s85, s87\n\t"
    "s_and_b32 s40, s40, 7\n\t"
    "s_and_b32 s41, s85, 24\n\t"
    "s_or_b32 s40, s40, s41\n\t"
    "s_lshl_b32 s40, s40, 7\n\t"
    "s_add_u32 s40, s40, s36\n\t"
    "s_addc_u32 s41, s37, 0\n\t"
    "s_add_u32 s69, s69, s75\n\t"
    "s_add_u32 s28, s18, 46137344\n\t"
    "s_addc_u32 s29, s19, 0\n\t"
    "s_load_dword s27, s[28:29], 69632\n\t"
    "s_mov_b32 s79, 0x7fffffff\n\t"
    "s_mov_b32 s68, 32\n\t"
    "s_mov_b32 s76, -65536\n\t"
    "s_mov_b32 s71, -65536\n\t"
    "s_mov_b32 s73, 3\n\t"
    "s_mov_b32 s78, -49152\n\t"
    "s_mov_b32 s72, -49152\n\t"
    "s_mov_b32 s74, 4\n\t"
    "s_mov_b32 m0, s69\n\t"
    "s_nop 0\n\t"
    "buffer_load_dwordx4 v18, s[40:43], s52 offen offset:0 lds\n\t"
    "buffer_load_dwordx4 v18, s[40:43], s53 offen offset:1024 lds\n\t"
    "buffer_load_dwordx4 v18, s[40:43], s54 offen offset:2048 lds\n\t"
    "buffer_load_dwordx4 v18, s[40:43], s55 offen offset:3072 lds\n\t"
    "s_bitset1_b32 m0, 14\n\t"
    "s_nop 0\n\t"
    "buffer_load_dwordx4 v18, s[40:43], s56 offen offset:0 lds\n\t"
    "buffer_load_dwordx4 v18, s[40:43], s57 offen offset:1024 lds\n\t"
    "buffer_load_dwordx4 v18, s[40:43], s58 offen offset:2048 lds\n\t"
    "buffer_load_dwordx4 v18, s[40:43], s59 offen offset:3072 lds\n\t"
    "s_add_u32 s85, s85, 1\n\t"
    "s_add_u32 s40, s85, s87\n\t"
    "s_and_b32 s40, s40, 7\n\t"
    "s_and_b32 s41, s85, 24\n\t"
    "s_or_b32 s40, s40, s41\n\t"
    "s_lshl_b32 s40, s40, 7\n\t"
    "s_add_u32 s40, s40, s36\n\t"
    "s_addc_u32 s41, s37, 0\n\t"
    "s_add_u32 s69, s69, s75\n\t"
    "s_mov_b32 m0, s69\n\t"
    "s_nop 0\n\t"
    "buffer_load_dwordx4 v18, s[40:43], s52 offen offset:0 lds\n\t"
    "buffer_load_dwordx4 v18, s[40:43], s53 offen offset:1024 lds\n\t"
    "buffer_load_dwordx4 v18, s[40:43], s54 offen offset:2048 lds\n\t"
    "buffer_load_dwordx4 v18, s[40:43], s55 offen offset:3072 lds\n\t"
    "s_bitset1_b32 m0, 14\n\t"
    "s_nop 0\n\t"
    "buffer_load_dwordx4 v18, s[40:43], s56 offen offset:0 lds\n\t"
    "buffer_load_dwordx4 v18, s[40:43], s57 offen offset:1024 lds\n\t"
    "buffer_load_dwordx4 v18, s[40:43], s58 offen offset:2048 lds\n\t"
    "buffer_load_dwordx4 v18, s[40:43], s59 offen offset:3072 lds\n\t"
    "s_add_u32 s85, s85, 1\n\t"
    "s_add_u32 s40, s85, s87\n\t"
    "s_and_b32 s40, s40, 7\n\t"
    "s_and_b32 s41, s85, 24\n\t"
    "s_or_b32 s40, s40, s41\n\t"
    "s_lshl_b32 s40, s40, 7\n\t"
    "s_add_u32 s40, s40, s36\n\t"
    "s_addc_u32 s41, s37, 0\n\t"
    "s_add_u32 s69, s69, s71\n\t"
    "v_lshrrev_b32 v3, 4, v0\n\t"
    "v_and_b32 v4, 3, v3\n\t"
    "v_lshl_add_u32 v3, s22, 4, v4\n\t"
    "s_movk_i32 s31, 2048\n\t"
    "v_mul_u32_u24 v3, s31, v3\n\t"
    "v_lshlrev_b32 v4, 1, v4\n\t"
    "v_and_b32 v5, 15, v0\n\t"
    "v_xor_b32 v4, v4, v5\n\t"
    "v_lshl_add_u32 v2, v4, 4, v3\n\t"
    "v_mov_b32 v19, v2\n\t"
    "v_xor_b32 v20, 128, v2\n\t"
    "s_lshl_b32 s31, s34, 8\n\t"
    "s_add_u32 s38, s18, s31\n\t"
    "s_addc_u32 s39, s19, 0\n\t"
    "s_mul_i32 s86, s87, 131072\n\t"
    "s_add_u32 s44, s38, s86\n\t"
    "s_addc_u32 s45, s39, 0\n\t"
    "s_mov_b32 s86, 0\n\t"
    "s_mov_b32 s46, 0x7fffffff\n\t"
    "s_mov_b32 s47, 0x00020000\n\t"
    "s_add_u32 s70, s23, 98304\n\t"
    "s_mov_b32 s77, 16384\n\t"
    "s_mov_b32 s60, 0\n\t"
    "s_mov_b32 s61, 7168\n\t"
    "s_mov_b32 s62, 14336\n\t"
    "s_mov_b32 s63, 21504\n\t"
    "s_lshl_b32 s31, %[b], 2\n\t"
    "s_add_u32 s31, s31, s22\n\t"
    "s_lshl_b32 s31, s31, 7\n\t"
    "s_add_u32 s31, s31, 16384\n\t"
    "v_mov_b32 v3, s31\n\t"
    "global_load_dword v28, v3, s[28:29] sc1\n\t"
    "v_lshrrev_b32 v3, 4, v1\n\t"
    "v_and_b32 v4, 6, v0\n\t"
    "v_xor_b32 v4, v4, v3\n\t"
    "v_lshlrev_b32 v4, 4, v4\n\t"
    "v_and_b32 v5, 15, v0\n\t"
    "v_lshl_add_u32 v4, v5, 7, v4\n\t"
    "s_lshl_b32 s31, s22, 13\n\t"
    "s_and_b32 s31, s31, 0x4000\n\t"
    "v_add_u32 v8, s31, v4\n\t"
    "v_xor_b32 v9, 64, v8\n\t"
    "v_bfe_u32 v4, v0, 2, 2\n\t"
    "v_lshl_add_u32 v3, v3, 3, v4\n\t"
    "v_lshlrev_b32 v3, 8, v3\n\t"
    "v_bfe_u32 v4, v0, 2, 3\n\t"
    "v_lshl_add_u32 v3, v4, 5, v3\n\t"
    "v_and_b32 v4, 3, v0\n\t"
    "v_lshl_add_u32 v3, v4, 3, v3\n\t"
    "v_add_u32 v10, 98304, v3\n\t"
    "s_and_b32 s31, s22, 1\n\t"
    "s_lshl_b32 s31, s31, 7\n\t"
    "v_xor_b32 v10, s31, v10\n\t"
    "v_xor_b32 v11, 32, v10\n\t"
    "v_xor_b32 v12, 64, v10\n\t"
    "v_xor_b32 v13, 96, v10\n\t"
    "s_lshr_b32 s31, s22, 1\n\t"
    "s_lshl_b32 s31, s31, 13\n\t"
    "s_and_b32 s98, s22, 1\n\t"
    "s_mul_i32 s98, s98, 262144\n\t"
    "s_add_u32 s31, s31, s98\n\t"
    "v_lshl_add_u32 v23, v1, 4, s31\n\t"
    "s_mul_i32 s35, s24, 0x80008000\n\t"
    "s_lshl_b32 s31, s34, 2\n\t"
    "s_add_u32 s31, s31, 56\n\t"
    "s_mul_i32 s98, s24, 88\n\t"
    "s_add_u32 s31, s31, s98\n\t"
    "s_mul_i32 s31, s31, 131072\n\t"
    "s_lshl_b32 s98, s33, 14\n\t"
    "s_add_u32 s31, s31, s98\n\t"
    "s_add_u32 s31, s31, 4194304\n\t"
    "s_add_u32 s48, s18, s31\n\t"
    "s_addc_u32 s49, s19, 0\n\t"
    "s_mov_b32 s50, 0x7fffffff\n\t"
    "s_mov_b32 s51, 0x00020000\n\t"
    "v_mov_b64 v[96:97], 0\n\t"
    "v_mov_b64 v[98:99], 0\n\t"
    "v_mov_b64 v[100:101], 0\n\t"
    "v_mov_b64 v[102:103], 0\n\t"
    "v_mov_b64 v[104:105], 0\n\t"
    "v_mov_b64 v[106:107], 0\n\t"
    "v_mov_b64 v[108:109], 0\n\t"
    "v_mov_b64 v[110:111], 0\n\t"
    "v_mov_b64 v[124:125], 0\n\t"
    "v_mov_b64 v[126:127], 0\n\t"
    "v_mov_b64 v[120:121], 0\n\t"
    "v_mov_b64 v[122:123], 0\n\t"
    "v_mov_b64 v[116:117], 0\n\t"
    "v_mov_b64 v[118:119], 0\n\t"
    "v_mov_b64 v[112:113], 0\n\t"
    "v_mov_b64 v[114:115], 0\n\t"
    "v_mov_b64 v[128:129], 0\n\t"
    "v_mov_b64 v[130:131], 0\n\t"
    "v_mov_b64 v[132:133], 0\n\t"
    "v_mov_b64 v[134:135], 0\n\t"
    "v_mov_b64 v[136:137], 0\n\t"
    "v_mov_b64 v[138:139], 0\n\t"
    "v_mov_b64 v[140:141], 0\n\t"
    "v_mov_b64 v[142:143], 0\n\t"
    "v_mov_b64 v[156:157], 0\n\t"
    "v_mov_b64 v[158:159], 0\n\t"
    "v_mov_b64 v[152:153], 0\n\t"
    "v_mov_b64 v[154:155], 0\n\t"
    "v_mov_b64 v[148:149], 0\n\t"
    "v_mov_b64 v[150:151], 0\n\t"
    "v_mov_b64 v[144:145], 0\n\t"
    "v_mov_b64 v[146:147], 0\n\t"
    "v_mov_b64 v[80:81], 0\n\t"
    "v_mov_b64 v[82:83], 0\n\t"
    "s_waitcnt lgkmcnt(0)\n\t"
    "s_mul_i32 s27, s27, 0x9E3779B1\n\t"
    "s_add_u32 s27, s27, 0x7F4A7C15\n\t"
    "s_cmp_lg_u32 s22, 0\n\t"
    "s_cbranch_scc1 Lsdlp%=\n\t"
    "v_lshlrev_b32 v5, 8, v1\n\t"
    "s_mov_b32 s84, 0x20000\n\t"
    "Lsplp%=:\n\t"
    "global_load_dword v25, v5, s[28:29] offset:0 sc1\n\t"
    "global_load_dword v26, v5, s[28:29] offset:128 sc1\n\t"
    "s_waitcnt vmcnt(0)\n\t"
    "v_cmp_ne_u32 vcc, s27, v25\n\t"
    "v_cmp_ne_u32 s[96:97], s27, v26\n\t"
    "s_nop 1\n\t"
    "s_or_b64 vcc, vcc, s[96:97]\n\t"
    "s_sub_u32 s84, s84, 1\n\t"
    "s_cbranch_scc1 Lsdlp%=\n\t"
    "s_nop 2\n\t"
    "s_cbranch_vccz Lsdlp%=\n\t"
    "s_sleep 4\n\t"
    "s_branch Lsplp%=\n\t"
    "Lsdlp%=:\n\t"
    "s_barrier\n\t"
    "s_mov_b32 m0, s70\n\t"
    "s_nop 0\n\t"
    "buffer_load_dwordx4 v19, s[44:47], s60 offen offset:0 sc1 lds\n\t"
    "buffer_load_dwordx4 v19, s[44:47], s61 offen offset:1024 sc1 lds\n\t"
    "buffer_load_dwordx4 v20, s[44:47], s62 offen offset:2048 sc1 lds\n\t"
    "buffer_load_dwordx4 v20, s[44:47], s63 offen offset:3072 sc1 lds\n\t"
    "s_add_u32 s86, s86, 1\n\t"
    "s_add_u32 s44, s86, s87\n\t"
    "s_and_b32 s44, s44, 7\n\t"
    "s_and_b32 s45, s86, 24\n\t"
    "s_or_b32 s44, s44, s45\n\t"
    "s_lshl_b32 s44, s44, 17\n\t"
    "s_add_u32 s44, s44, s38\n\t"
    "s_addc_u32 s45, s39, 0\n\t"
    "s_add_u32 s70, s70, s77\n\t"
    "s_mov_b32 m0, s70\n\t"
    "s_nop 0\n\t"
    "buffer_load_dwordx4 v19, s[44:47], s60 offen offset:0 sc1 lds\n\t"
    "buffer_load_dwordx4 v19, s[44:47], s61 offen offset:1024 sc1 lds\n\t"
    "buffer_load_dwordx4 v20, s[44:47], s62 offen offset:2048 sc1 lds\n\t"
    "buffer_load_dwordx4 v20, s[44:47], s63 offen offset:3072 sc1 lds\n\t"
    "s_add_u32 s86, s86, 1\n\t"
    "s_add_u32 s44, s86, s87\n\t"
    "s_and_b32 s44, s44, 7\n\t"
    "s_and_b32 s45, s86, 24\n\t"
    "s_or_b32 s44, s44, s45\n\t"
    "s_lshl_b32 s44, s44, 17\n\t"
    "s_add_u32 s44, s44, s38\n\t"
    "s_addc_u32 s45, s39, 0\n\t"
    "s_add_u32 s70, s70, s77\n\t"
    "s_waitcnt vmcnt(4)\n\t"
    "s_barrier\n\t"
    "ds_read_b64_tr_b16 a[0:1], v10 offset:0\n\t"
    "ds_read_b64_tr_b16 a[2:3], v10 offset:1024\n\t"
    "ds_read_b64_tr_b16 a[4:5], v11 offset:0\n\t"
    "ds_read_b64_tr_b16 a[6:7], v11 offset:1024\n\t"
    "ds_read_b64_tr_b16 a[8:9], v12 offset:0\n\t"
    "ds_read_b64_tr_b16 a[10:11], v12 offset:1024\n\t"
    "ds_read_b64_tr_b16 a[12:13], v13 offset:0\n\t"
    "ds_read_b64_tr_b16 a[14:15], v13 offset:1024\n\t"
    "ds_read_b64_tr_b16 a[16:17], v10 offset:8192\n\t"
    "ds_read_b64_tr_b16 a[18:19], v10 offset:9216\n\t"
    "ds_read_b64_tr_b16 a[20:21], v11 offset:8192\n\t"
    "ds_read_b64_tr_b16 a[22:23], v11 offset:9216\n\t"
    "ds_read_b64_tr_b16 a[24:25], v12 offset:8192\n\t"
    "ds_read_b64_tr_b16 a[26:27], v12 offset:9216\n\t"
    "ds_read_b64_tr_b16 a[28:29], v13 offset:8192\n\t"
    "ds_read_b64_tr_b16 a[30:31], v13 offset:9216\n\t"
    "ds_read_b128 a[64:67], v8 offset:0\n\t"
    "ds_read_b128 a[80:83], v9 offset:0\n\t"
    "ds_read_b128 a[68:71], v8 offset:2048\n\t"
    "ds_read_b128 a[84:87], v9 offset:2048\n\t"
    "ds_read_b128 a[72:75], v8 offset:4096\n\t"
    "ds_read_b128 a[88:91], v9 offset:4096\n\t"
    "ds_read_b128 a[76:79], v8 offset:6144\n\t"
    "ds_read_b128 a[92:95], v9 offset:6144\n\t"
    "s_waitcnt lgkmcnt(0)\n\t"
    "v_mfma_f32_16x16x32_bf16 v[32:35], a[0:3], a[64:67], 0\n\t"
    "ds_read_b128 a[96:99], v8 offset:8192\n\t"
    "s_mov_b32 m0, s70\n\t"
    "v_mfma_f32_16x16x32_bf16 v[32:35], a[16:19], a[80:83], v[32:35]\n\t"
    "ds_read_b128 a[112:115], v9 offset:8192\n\t"
    "buffer_load_dwordx4 v19, s[44:47], s60 offen offset:0 sc1 lds\n\t"
    "v_mfma_f32_16x16x32_bf16 v[36:39], a[20:23], a[80:83], 0\n\t"
    "ds_read_b128 a[100:103], v8 offset:10240\n\t"
    "v_mfma_f32_16x16x32_bf16 v[36:39], a[4:7], a[64:67], v[36:39]\n\t"
    "ds_read_b128 a[116:119], v9 offset:10240\n\t"
    "buffer_load_dwordx4 v19, s[44:47], s61 offen offset:1024 sc1 lds\n\t"
    "v_mfma_f32_16x16x32_bf16 v[40:43], a[8:11], a[64:67], 0\n\t"
    "ds_read_b128 a[104:107], v8 offset:12288\n\t"
    "v_mfma_f32_16x16x32_bf16 v[40:43], a[24:27], a[80:83], v[40:43]\n\t"
    "ds_read_b128 a[120:123], v9 offset:12288\n\t"
    "buffer_load_dwordx4 v20, s[44:47], s62 offen offset:2048 sc1 lds\n\t"
    "v_mfma_f32_16x16x32_bf16 v[44:47], a[28:31], a[80:83], 0\n\t"
    "ds_read_b128 a[108:111], v8 offset:14336\n\t"
    "v_mfma_f32_16x16x32_bf16 v[44:47], a[12:15], a[64:67], v[44:47]\n\t"
    "buffer_load_dwordx4 v20, s[44:47], s63 offen offset:3072 sc1 lds\n\t"
    "s_add_u32 s86, s86, 1\n\t"
    "s_add_u32 s44, s86, s87\n\t"
    "s_and_b32 s44, s44, 7\n\t"
    "s_and_b32 s45, s86, 24\n\t"
    "s_or_b32 s44, s44, s45\n\t"
    "s_lshl_b32 s44, s44, 17\n\t"
    "s_add_u32 s44, s44, s38\n\t"
    "v_mfma_f32_16x16x32_bf16 v[60:63], a[12:15], a[68:71], 0\n\t"
    "s_addc_u32 s45, s39, 0\n\t"
    "s_add_u32 s70, s70, s77\n\t"
    "s_mov_b32 m0, s70\n\t"
    "v_mfma_f32_16x16x32_bf16 v[60:63], a[28:31], a[84:87], v[60:63]\n\t"
    "ds_read_b128 a[124:127], v9 offset:14336\n\t"
    "buffer_load_dwordx4 v19, s[44:47], s60 offen offset:0 sc1 lds\n\t"
    "v_mfma_f32_16x16x32_bf16 v[56:59], a[24:27], a[84:87], 0\n\t"
    "v_mfma_f32_16x16x32_bf16 v[56:59], a[8:11], a[68:71], v[56:59]\n\t"
    "buffer_load_dwordx4 v19, s[44:47], s61 offen offset:1024 sc1 lds\n\t"
    "v_mfma_f32_16x16x32_bf16 v[52:55], a[4:7], a[68:71], 0\n\t"
    "v_mfma_f32_16x16x32_bf16 v[52:55], a[20:23], a[84:87], v[52:55]\n\t"
    "buffer_load_dwordx4 v20, s[44:47], s62 offen offset:2048 sc1 lds\n\t"
    "v_mfma_f32_16x16x32_bf16 v[48:51], a[16:19], a[84:87], 0\n\t"
    "v_mfma_f32_16x16x32_bf16 v[48:51], a[0:3], a[68:71], v[48:51]\n\t"
    "buffer_load_dwordx4 v20, s[44:47], s63 offen offset:3072 sc1 lds\n\t"
    "v_mfma_f32_16x16x32_bf16 v[64:67], a[0:3], a[72:75], 0\n\t"
    "s_add_u32 s86, s86, 1\n\t"
    "s_add_u32 s44, s86, s87\n\t"
    "s_and_b32 s44, s44, 7\n\t"
    "v_mfma_f32_16x16x32_bf16 v[64:67], a[16:19], a[88:91], v[64:67]\n\t"
    "s_and_b32 s45, s86, 24\n\t"
    "s_or_b32 s44, s44, s45\n\t"
    "s_lshl_b32 s44, s44, 17\n\t"
    "v_mfma_f32_16x16x32_bf16 v[68:71], a[20:23], a[88:91], 0\n\t"
    "s_add_u32 s44, s44, s38\n\t"
    "s_addc_u32 s45, s39, 0\n\t"
    "s_add_u32 s70, s70, s72\n\t"
    "v_mfma_f32_16x16x32_bf16 v[68:71], a[4:7], a[72:75], v[68:71]\n\t"
    "s_sub_u32 s73, s73, 1\n\t"
    "s_cmp_eq_u32 s73, 0\n\t"
    "v_mfma_f32_16x16x32_bf16 v[72:75], a[8:11], a[72:75], 0\n\t"
    "s_cselect_b32 s71, s76, s75\n\t"
    "v_mfma_f32_16x16x32_bf16 v[72:75], a[24:27], a[88:91], v[72:75]\n\t"
    "s_cselect_b32 s73, 3, s73\n\t"
    "v_add_u32 v8, s71, v8\n\t"
    "v_mfma_f32_16x16x32_bf16 v[76:79], a[28:31], a[88:91], 0\n\t"
    "v_add_u32 v9, s71, v9\n\t"
    "v_mfma_f32_16x16x32_bf16 v[76:79], a[12:15], a[72:75], v[76:79]\n\t"
    "s_sub_u32 s74, s74, 1\n\t"
    "v_mfma_f32_16x16x32_bf16 v[92:95], a[12:15], a[76:79], 0\n\t"
    "s_cmp_eq_u32 s74, 0\n\t"
    "s_cselect_b32 s72, s78, s77\n\t"
    "v_mfma_f32_16x16x32_bf16 v[92:95], a[28:31], a[92:95], v[92:95]\n\t"
    "s_cselect_b32 s74, 4, s74\n\t"
    "v_mfma_f32_16x16x32_bf16 v[88:91], a[24:27], a[92:95], 0\n\t"
    "v_add_u32 v10, s72, v10\n\t"
    "v_mfma_f32_16x16x32_bf16 v[88:91], a[8:11], a[76:79], v[88:91]\n\t"
    "v_add_u32 v11, s72, v11\n\t"
    "v_add_u32 v12, s72, v12\n\t"
    "v_mfma_f32_16x16x32_bf16 v[84:87], a[4:7], a[76:79], 0\n\t"
    "v_add_u32 v13, s72, v13\n\t"
    "v_mfma_f32_16x16x32_bf16 v[84:87], a[20:23], a[92:95], v[84:87]\n\t"
    "s_sub_u32 s68, s68, 1\n\t"
    "s_waitcnt vmcnt(8)\n\t"
    "Lloopp%=:\n\t"
    "v_mfma_f32_16x16x32_bf16 v[80:83], a[16:19], a[92:95], v[80:83]\n\t"
    "s_waitcnt vmcnt(12) lgkmcnt(0)\n\t"
    "v_mfma_f32_16x16x32_bf16 v[80:83], a[0:3], a[76:79], v[80:83]\n\t"
    "s_barrier\n\t"
    "v_mfma_f32_16x16x32_bf16 v[96:99], a[0:3], a[96:99], v[96:99]\n\t"
    "ds_read_b64_tr_b16 a[32:33], v10 offset:0\n\t"
    "s_cmp_ge_u32 s68, 4\n\t"
    "v_mfma_f32_16x16x32_bf16 v[96:99], a[16:19], a[112:115], v[96:99]\n\t"
    "ds_read_b64_tr_b16 a[34:35], v10 offset:1024\n\t"
    "s_cselect_b32 s46, s79, 0\n\t"
    "v_mfma_f32_16x16x32_bf16 v[100:103], a[20:23], a[112:115], v[100:103]\n\t"
    "ds_read_b64_tr_b16 a[36:37], v11 offset:0\n\t"
    "s_mov_b32 m0, s70\n\t"
    "v_mfma_f32_16x16x32_bf16 v[100:103], a[4:7], a[96:99], v[100:103]\n\t"
    "buffer_load_dwordx4 v19, s[44:47], s60 offen offset:0 sc1 lds\n\t"
    "v_mfma_f32_16x16x32_bf16 v[104:107], a[8:11], a[96:99], v[104:107]\n\t"
    "ds_read_b64_tr_b16 a[38:39], v11 offset:1024\n\t"
    "ds_read_b64_tr_b16 a[40:41], v12 offset:0\n\t"
    "v_mfma_f32_16x16x32_bf16 v[104:107], a[24:27], a[112:115], v[104:107]\n\t"
    "ds_read_b64_tr_b16 a[42:43], v12 offset:1024\n\t"
    "ds_read_b64_tr_b16 a[44:45], v13 offset:0\n\t"
    "v_mfma_f32_16x16x32_bf16 v[108:111], a[28:31], a[112:115], v[108:111]\n\t"
    "ds_read_b64_tr_b16 a[46:47], v13 offset:1024\n\t"
    "ds_read_b64_tr_b16 a[48:49], v10 offset:8192\n\t"
    "v_mfma_f32_16x16x32_bf16 v[108:111], a[12:15], a[96:99], v[108:111]\n\t"
    "buffer_load_dwordx4 v19, s[44:47], s61 offen offset:1024 sc1 lds\n\t"
    "v_mfma_f32_16x16x32_bf16 v[124:127], a[12:15], a[100:103], v[124:127]\n\t"
    "ds_read_b64_tr_b16 a[50:51], v10 offset:9216\n\t"
    "ds_read_b64_tr_b16 a[52:53], v11 offset:8192\n\t"
    "v_mfma_f32_16x16x32_bf16 v[124:127], a[28:31], a[116:119], v[124:127]\n\t"
    "ds_read_b64_tr_b16 a[54:55], v11 offset:9216\n\t"
    "ds_read_b64_tr_b16 a[56:57], v12 offset:8192\n\t"
    "v_mfma_f32_16x16x32_bf16 v[120:123], a[24:27], a[116:119], v[120:123]\n\t"
    "ds_read_b64_tr_b16 a[58:59], v12 offset:9216\n\t"
    "ds_read_b64_tr_b16 a[60:61], v13 offset:8192\n\t"
    "v_mfma_f32_16x16x32_bf16 v[120:123], a[8:11], a[100:103], v[120:123]\n\t"
    "ds_read_b64_tr_b16 a[62:63], v13 offset:9216\n\t"
    "s_add_u32 s86, s86, 1\n\t"
    "v_mfma_f32_16x16x32_bf16 v[116:119], a[4:7], a[100:103], v[116:119]\n\t"
    "buffer_load_dwordx4 v20, s[44:47], s62 offen offset:2048 sc1 lds\n\t"
    "v_mfma_f32_16x16x32_bf16 v[116:119], a[20:23], a[116:119], v[116:119]\n\t"
    "ds_read_b128 a[64:67], v8 offset:0\n\t"
    "s_add_u32 s82, s86, s87\n\t"
    "v_mfma_f32_16x16x32_bf16 v[112:115], a[16:19], a[116:119], v[112:115]\n\t"
    "ds_read_b128 a[80:83], v9 offset:0\n\t"
    "s_and_b32 s82, s82, 7\n\t"
    "v_mfma_f32_16x16x32_bf16 v[112:115], a[0:3], a[100:103], v[112:115]\n\t"
    "ds_read_b128 a[68:71], v8 offset:2048\n\t"
    "s_and_b32 s83, s86, 24\n\t"
    "v_mfma_f32_16x16x32_bf16 v[128:131], a[0:3], a[104:107], v[128:131]\n\t"
    "ds_read_b128 a[84:87], v9 offset:2048\n\t"
    "s_or_b32 s82, s82, s83\n\t"
    "v_mfma_f32_16x16x32_bf16 v[128:131], a[16:19], a[120:123], v[128:131]\n\t"
    "buffer_load_dwordx4 v20, s[44:47], s63 offen offset:3072 sc1 lds\n\t"
    "v_mfma_f32_16x16x32_bf16 v[132:135], a[20:23], a[120:123], v[132:135]\n\t"
    "ds_read_b128 a[72:75], v8 offset:4096\n\t"
    "s_lshl_b32 s82, s82, 17\n\t"
    "v_mfma_f32_16x16x32_bf16 v[132:135], a[4:7], a[104:107], v[132:135]\n\t"
    "ds_read_b128 a[88:91], v9 offset:4096\n\t"
    "s_add_u32 s82, s82, s38\n\t"
    "v_mfma_f32_16x16x32_bf16 v[136:139], a[8:11], a[104:107], v[136:139]\n\t"
    "ds_read_b128 a[76:79], v8 offset:6144\n\t"
    "s_addc_u32 s83, s39, 0\n\t"
    "v_mfma_f32_16x16x32_bf16 v[136:139], a[24:27], a[120:123], v[136:139]\n\t"
    "ds_read_b128 a[92:95], v9 offset:6144\n\t"
    "s_mov_b32 m0, s69\n\t"
    "v_mfma_f32_16x16x32_bf16 v[140:143], a[28:31], a[120:123], v[140:143]\n\t"
    "buffer_load_dwordx4 v18, s[40:43], s52 offen offset:0 lds\n\t"
    "v_mfma_f32_16x16x32_bf16 v[140:143], a[12:15], a[104:107], v[140:143]\n\t"
    "s_mov_b64 s[44:45], s[82:83]\n\t"
    "s_add_u32 s70, s70, s72\n\t"
    "v_mfma_f32_16x16x32_bf16 v[156:159], a[12:15], a[108:111], v[156:159]\n\t"
    "s_sub_u32 s73, s73, 1\n\t"
    "s_cmp_eq_u32 s73, 0\n\t"
    "v_mfma_f32_16x16x32_bf16 v[156:159], a[28:31], a[124:127], v[156:159]\n\t"
    "s_cselect_b32 s64, s76, s75\n\t"
    "s_cselect_b32 s73, 3, s73\n\t"
    "v_mfma_f32_16x16x32_bf16 v[152:155], a[24:27], a[124:127], v[152:155]\n\t"
    "s_sub_u32 s74, s74, 1\n\t"
    "s_cmp_eq_u32 s74, 0\n\t"
    "v_mfma_f32_16x16x32_bf16 v[152:155], a[8:11], a[108:111], v[152:155]\n\t"
    "buffer_load_dwordx4 v18, s[40:43], s53 offen offset:1024 lds\n\t"
    "v_mfma_f32_16x16x32_bf16 v[148:151], a[4:7], a[108:111], v[148:151]\n\t"
    "s_cselect_b32 s65, s78, s77\n\t"
    "s_cselect_b32 s74, 4, s74\n\t"
    "v_mfma_f32_16x16x32_bf16 v[148:151], a[20:23], a[124:127], v[148:151]\n\t"
    "s_add_u32 s85, s85, 1\n\t"
    "s_add_u32 s82, s85, s87\n\t"
    "v_mfma_f32_16x16x32_bf16 v[144:147], a[16:19], a[124:127], v[144:147]\n\t"
    "s_and_b32 s82, s82, 7\n\t"
    "s_and_b32 s83, s85, 24\n\t"
    "v_mfma_f32_16x16x32_bf16 v[144:147], a[0:3], a[108:111], v[144:147]\n\t"
    "s_or_b32 s82, s82, s83\n\t"
    "s_waitcnt lgkmcnt(0)\n\t"
    "v_mfma_f32_16x16x32_bf16 v[32:35], a[32:35], a[64:67], v[32:35]\n\t"
    "buffer_load_dwordx4 v18, s[40:43], s54 offen offset:2048 lds\n\t"
    "v_mfma_f32_16x16x32_bf16 v[32:35], a[48:51], a[80:83], v[32:35]\n\t"
    "ds_read_b128 a[96:99], v8 offset:8192\n\t"
    "s_lshl_b32 s82, s82, 7\n\t"
    "v_mfma_f32_16x16x32_bf16 v[36:39], a[52:55], a[80:83], v[36:39]\n\t"
    "ds_read_b128 a[112:115], v9 offset:8192\n\t"
    "s_add_u32 s82, s82, s36\n\t"
    "v_mfma_f32_16x16x32_bf16 v[36:39], a[36:39], a[64:67], v[36:39]\n\t"
    "ds_read_b128 a[100:103], v8 offset:10240\n\t"
    "s_addc_u32 s83, s37, 0\n\t"
    "v_mfma_f32_16x16x32_bf16 v[40:43], a[40:43], a[64:67], v[40:43]\n\t"
    "buffer_load_dwordx4 v18, s[40:43], s55 offen offset:3072 lds\n\t"
    "v_mfma_f32_16x16x32_bf16 v[40:43], a[56:59], a[80:83], v[40:43]\n\t"
    "ds_read_b128 a[116:119], v9 offset:10240\n\t"
    "v_add_u32 v10, s65, v10\n\t"
    "v_mfma_f32_16x16x32_bf16 v[44:47], a[60:63], a[80:83], v[44:47]\n\t"
    "ds_read_b128 a[104:107], v8 offset:12288\n\t"
    "v_add_u32 v11, s65, v11\n\t"
    "v_mfma_f32_16x16x32_bf16 v[44:47], a[44:47], a[64:67], v[44:47]\n\t"
    "ds_read_b128 a[120:123], v9 offset:12288\n\t"
    "v_add_u32 v12, s65, v12\n\t"
    "v_mfma_f32_16x16x32_bf16 v[60:63], a[44:47], a[68:71], v[60:63]\n\t"
    "ds_read_b128 a[108:111], v8 offset:14336\n\t"
    "s_bitset1_b32 m0, 14\n\t"
    "v_mfma_f32_16x16x32_bf16 v[60:63], a[60:63], a[84:87], v[60:63]\n\t"
    "buffer_load_dwordx4 v18, s[40:43], s56 offen offset:0 lds\n\t"
    "v_mfma_f32_16x16x32_bf16 v[56:59], a[56:59], a[84:87], v[56:59]\n\t"
    "ds_read_b128 a[124:127], v9 offset:14336\n\t"
    "v_add_u32 v13, s65, v13\n\t"
    "v_mfma_f32_16x16x32_bf16 v[56:59], a[40:43], a[68:71], v[56:59]\n\t"
    "v_add_u32 v8, s64, v8\n\t"
    "v_add_u32 v9, s64, v9\n\t"
    "v_mfma_f32_16x16x32_bf16 v[52:55], a[36:39], a[68:71], v[52:55]\n\t"
    "v_mfma_f32_16x16x32_bf16 v[52:55], a[52:55], a[84:87], v[52:55]\n\t"
    "v_mfma_f32_16x16x32_bf16 v[48:51], a[48:51], a[84:87], v[48:51]\n\t"
    "buffer_load_dwordx4 v18, s[40:43], s57 offen offset:1024 lds\n\t"
    "v_mfma_f32_16x16x32_bf16 v[48:51], a[32:35], a[68:71], v[48:51]\n\t"
    "v_mfma_f32_16x16x32_bf16 v[64:67], a[32:35], a[72:75], v[64:67]\n\t"
    "v_mfma_f32_16x16x32_bf16 v[64:67], a[48:51], a[88:91], v[64:67]\n\t"
    "v_mfma_f32_16x16x32_bf16 v[68:71], a[52:55], a[88:91], v[68:71]\n\t"
    "v_mfma_f32_16x16x32_bf16 v[68:71], a[36:39], a[72:75], v[68:71]\n\t"
    "buffer_load_dwordx4 v18, s[40:43], s58 offen offset:2048 lds\n\t"
    "v_mfma_f32_16x16x32_bf16 v[72:75], a[40:43], a[72:75], v[72:75]\n\t"
    "v_mfma_f32_16x16x32_bf16 v[72:75], a[56:59], a[88:91], v[72:75]\n\t"
    "v_mfma_f32_16x16x32_bf16 v[76:79], a[60:63], a[88:91], v[76:79]\n\t"
    "v_mfma_f32_16x16x32_bf16 v[76:79], a[44:47], a[72:75], v[76:79]\n\t"
    "v_mfma_f32_16x16x32_bf16 v[92:95], a[44:47], a[76:79], v[92:95]\n\t"
    "buffer_load_dwordx4 v18, s[40:43], s59 offen offset:3072 lds\n\t"
    "v_mfma_f32_16x16x32_bf16 v[92:95], a[60:63], a[92:95], v[92:95]\n\t"
    "s_mov_b64 s[40:41], s[82:83]\n\t"
    "s_add_u32 s69, s69, s71\n\t"
    "v_mfma_f32_16x16x32_bf16 v[88:91], a[56:59], a[92:95], v[88:91]\n\t"
    "s_sub_u32 s68, s68, 1\n\t"
    "s_cmp_lg_u32 s68, 2\n\t"
    "v_mfma_f32_16x16x32_bf16 v[88:91], a[40:43], a[76:79], v[88:91]\n\t"
    "v_mfma_f32_16x16x32_bf16 v[84:87], a[36:39], a[76:79], v[84:87]\n\t"
    "v_mfma_f32_16x16x32_bf16 v[84:87], a[52:55], a[92:95], v[84:87]\n\t"
    "s_cbranch_scc0 Loutp%=\n\t"
    "v_mfma_f32_16x16x32_bf16 v[80:83], a[48:51], a[92:95], v[80:83]\n\t"
    "s_waitcnt vmcnt(12) lgkmcnt(0)\n\t"
    "v_mfma_f32_16x16x32_bf16 v[80:83], a[32:35], a[76:79], v[80:83]\n\t"
    "s_barrier\n\t"
    "v_mfma_f32_16x16x32_bf16 v[96:99], a[32:35], a[96:99], v[96:99]\n\t"
    "ds_read_b64_tr_b16 a[0:1], v10 offset:0\n\t"
    "s_cmp_ge_u32 s68, 4\n\t"
    "v_mfma_f32_16x16x32_bf16 v[96:99], a[48:51], a[112:115], v[96:99]\n\t"
    "ds_read_b64_tr_b16 a[2:3], v10 offset:1024\n\t"
    "s_cselect_b32 s46, s79, 0\n\t"
    "v_mfma_f32_16x16x32_bf16 v[100:103], a[52:55], a[112:115], v[100:103]\n\t"
    "ds_read_b64_tr_b16 a[4:5], v11 offset:0\n\t"
    "s_mov_b32 m0, s70\n\t"
    "v_mfma_f32_16x16x32_bf16 v[100:103], a[36:39], a[96:99], v[100:103]\n\t"
    "buffer_load_dwordx4 v19, s[44:47], s60 offen offset:0 sc1 lds\n\t"
    "v_mfma_f32_16x16x32_bf16 v[104:107], a[40:43], a[96:99], v[104:107]\n\t"
    "ds_read_b64_tr_b16 a[6:7], v11 offset:1024\n\t"
    "ds_read_b64_tr_b16 a[8:9], v12 offset:0\n\t"
    "v_mfma_f32_16x16x32_bf16 v[104:107], a[56:59], a[112:115], v[104:107]\n\t"
    "ds_read_b64_tr_b16 a[10:11], v12 offset:1024\n\t"
    "ds_read_b64_tr_b16 a[12:13], v13 offset:0\n\t"
    "v_mfma_f32_16x16x32_bf16 v[108:111], a[60:63], a[112:115], v[108:111]\n\t"
    "ds_read_b64_tr_b16 a[14:15], v13 offset:1024\n\t"
    "ds_read_b64_tr_b16 a[16:17], v10 offset:8192\n\t"
    "v_mfma_f32_16x16x32_bf16 v[108:111], a[44:47], a[96:99], v[108:111]\n\t"
    "buffer_load_dwordx4 v19, s[44:47], s61 offen offset:1024 sc1 lds\n\t"
    "v_mfma_f32_16x16x32_bf16 v[124:127], a[44:47], a[100:103], v[124:127]\n\t"
    "ds_read_b64_tr_b16 a[18:19], v10 offset:9216\n\t"
    "ds_read_b64_tr_b16 a[20:21], v11 offset:8192\n\t"
    "v_mfma_f32_16x16x32_bf16 v[124:127], a[60:63], a[116:119], v[124:127]\n\t"
    "ds_read_b64_tr_b16 a[22:23], v11 offset:9216\n\t"
    "ds_read_b64_tr_b16 a[24:25], v12 offset:8192\n\t"
    "v_mfma_f32_16x16x32_bf16 v[120:123], a[56:59], a[116:119], v[120:123]\n\t"
    "ds_read_b64_tr_b16 a[26:27], v12 offset:9216\n\t"
    "ds_read_b64_tr_b16 a[28:29], v13 offset:8192\n\t"
    "v_mfma_f32_16x16x32_bf16 v[120:123], a[40:43], a[100:103], v[120:123]\n\t"
    "ds_read_b64_tr_b16 a[30:31], v13 offset:9216\n\t"
    "s_add_u32 s86, s86, 1\n\t"
    "v_mfma_f32_16x16x32_bf16 v[116:119], a[36:39], a[100:103], v[116:119]\n\t"
    "buffer_load_dwordx4 v20, s[44:47], s62 offen offset:2048 sc1 lds\n\t"
    "v_mfma_f32_16x16x32_bf16 v[116:119], a[52:55], a[116:119], v[116:119]\n\t"
    "ds_read_b128 a[64:67], v8 offset:0\n\t"
    "s_add_u32 s82, s86, s87\n\t"
    "v_mfma_f32_16x16x32_bf16 v[112:115], a[48:51], a[116:119], v[112:115]\n\t"
    "ds_read_b128 a[80:83], v9 offset:0\n\t"
    "s_and_b32 s82, s82, 7\n\t"
    "v_mfma_f32_16x16x32_bf16 v[112:115], a[32:35], a[100:103], v[112:115]\n\t"
    "ds_read_b128 a[68:71], v8 offset:2048\n\t"
    "s_and_b32 s83, s86, 24\n\t"
    "v_mfma_f32_16x16x32_bf16 v[128:131], a[32:35], a[104:107], v[128:131]\n\t"
    "ds_read_b128 a[84:87], v9 offset:2048\n\t"
    "s_or_b32 s82, s82, s83\n\t"
    "v_mfma_f32_16x16x32_bf16 v[128:131], a[48:51], a[120:123], v[128:131]\n\t"
    "buffer_load_dwordx4 v20, s[44:47], s63 offen offset:3072 sc1 lds\n\t"
    "v_mfma_f32_16x16x32_bf16 v[132:135], a[52:55], a[120:123], v[132:135]\n\t"
    "ds_read_b128 a[72:75], v8 offset:4096\n\t"
    "s_lshl_b32 s82, s82, 17\n\t"
    "v_mfma_f32_16x16x32_bf16 v[132:135], a[36:39], a[104:107], v[132:135]\n\t"
    "ds_read_b128 a[88:91], v9 offset:4096\n\t"
    "s_add_u32 s82, s82, s38\n\t"
    "v_mfma_f32_16x16x32_bf16 v[136:139], a[40:43], a[104:107], v[136:139]\n\t"
    "ds_read_b128 a[76:79], v8 offset:6144\n\t"
    "s_addc_u32 s83, s39, 0\n\t"
    "v_mfma_f32_16x16x32_bf16 v[136:139], a[56:59], a[120:123], v[136:139]\n\t"
    "ds_read_b128 a[92:95], v9 offset:6144\n\t"
    "s_mov_b32 m0, s69\n\t"
    "v_mfma_f32_16x16x32_bf16 v[140:143], a[60:63], a[120:123], v[140:143]\n\t"
    "buffer_load_dwordx4 v18, s[40:43], s52 offen offset:0 lds\n\t"
    "v_mfma_f32_16x16x32_bf16 v[140:143], a[44:47], a[104:107], v[140:143]\n\t"
    "s_mov_b64 s[44:45], s[82:83]\n\t"
    "s_add_u32 s70, s70, s65\n\t"
    "v_mfma_f32_16x16x32_bf16 v[156:159], a[44:47], a[108:111], v[156:159]\n\t"
    "s_sub_u32 s73, s73, 1\n\t"
    "s_cmp_eq_u32 s73, 0\n\t"
    "v_mfma_f32_16x16x32_bf16 v[156:159], a[60:63], a[124:127], v[156:159]\n\t"
    "s_cselect_b32 s71, s76, s75\n\t"
    "s_cselect_b32 s73, 3, s73\n\t"
    "v_mfma_f32_16x16x32_bf16 v[152:155], a[56:59], a[124:127], v[152:155]\n\t"
    "s_sub_u32 s74, s74, 1\n\t"
    "s_cmp_eq_u32 s74, 0\n\t"
    "v_mfma_f32_16x16x32_bf16 v[152:155], a[40:43], a[108:111], v[152:155]\n\t"
    "buffer_load_dwordx4 v18, s[40:43], s53 offen offset:1024 lds\n\t"
    "v_mfma_f32_16x16x32_bf16 v[148:151], a[36:39], a[108:111], v[148:151]\n\t"
    "s_cselect_b32 s72, s78, s77\n\t"
    "s_cselect_b32 s74, 4, s74\n\t"
    "v_mfma_f32_16x16x32_bf16 v[148:151], a[52:55], a[124:127], v[148:151]\n\t"
    "s_add_u32 s85, s85, 1\n\t"
    "s_add_u32 s82, s85, s87\n\t"
    "v_mfma_f32_16x16x32_bf16 v[144:147], a[48:51], a[124:127], v[144:147]\n\t"
    "s_and_b32 s82, s82, 7\n\t"
    "s_and_b32 s83, s85, 24\n\t"
    "v_mfma_f32_16x16x32_bf16 v[144:147], a[32:35], a[108:111], v[144:147]\n\t"
    "s_or_b32 s82, s82, s83\n\t"
    "s_waitcnt lgkmcnt(0)\n\t"
    "v_mfma_f32_16x16x32_bf16 v[32:35], a[0:3], a[64:67], v[32:35]\n\t"
    "buffer_load_dwordx4 v18, s[40:43], s54 offen offset:2048 lds\n\t"
    "v_mfma_f32_16x16x32_bf16 v[32:35], a[16:19], a[80:83], v[32:35]\n\t"
    "ds_read_b128 a[96:99], v8 offset:8192\n\t"
    "s_lshl_b32 s82, s82, 7\n\t"
    "v_mfma_f32_16x16x32_bf16 v[36:39], a[20:23], a[80:83], v[36:39]\n\t"
    "ds_read_b128 a[112:115], v9 offset:8192\n\t"
    "s_add_u32 s82, s82, s36\n\t"
    "v_mfma_f32_16x16x32_bf16 v[36:39], a[4:7], a[64:67], v[36:39]\n\t"
    "ds_read_b128 a[100:103], v8 offset:10240\n\t"
    "s_addc_u32 s83, s37, 0\n\t"
    "v_mfma_f32_16x16x32_bf16 v[40:43], a[8:11], a[64:67], v[40:43]\n\t"
    "buffer_load_dwordx4 v18, s[40:43], s55 offen offset:3072 lds\n\t"
    "v_mfma_f32_16x16x32_bf16 v[40:43], a[24:27], a[80:83], v[40:43]\n\t"
    "ds_read_b128 a[116:119], v9 offset:10240\n\t"
    "v_add_u32 v10, s72, v10\n\t"
    "v_mfma_f32_16x16x32_bf16 v[44:47], a[28:31], a[80:83], v[44:47]\n\t"
    "ds_read_b128 a[104:107], v8 offset:12288\n\t"
    "v_add_u32 v11, s72, v11\n\t"
    "v_mfma_f32_16x16x32_bf16 v[44:47], a[12:15], a[64:67], v[44:47]\n\t"
    "ds_read_b128 a[120:123], v9 offset:12288\n\t"
    "v_add_u32 v12, s72, v12\n\t"
    "v_mfma_f32_16x16x32_bf16 v[60:63], a[12:15], a[68:71], v[60:63]\n\t"
    "ds_read_b128 a[108:111], v8 offset:14336\n\t"
    "s_bitset1_b32 m0, 14\n\t"
    "v_mfma_f32_16x16x32_bf16 v[60:63], a[28:31], a[84:87], v[60:63]\n\t"
    "buffer_load_dwordx4 v18, s[40:43], s56 offen offset:0 lds\n\t"
    "v_mfma_f32_16x16x32_bf16 v[56:59], a[24:27], a[84:87], v[56:59]\n\t"
    "ds_read_b128 a[124:127], v9 offset:14336\n\t"
    "v_add_u32 v13, s72, v13\n\t"
    "v_mfma_f32_16x16x32_bf16 v[56:59], a[8:11], a[68:71], v[56:59]\n\t"
    "v_add_u32 v8, s71, v8\n\t"
    "v_add_u32 v9, s71, v9\n\t"
    "v_mfma_f32_16x16x32_bf16 v[52:55], a[4:7], a[68:71], v[52:55]\n\t"
    "v_mfma_f32_16x16x32_bf16 v[52:55], a[20:23], a[84:87], v[52:55]\n\t"
    "v_mfma_f32_16x16x32_bf16 v[48:51], a[16:19], a[84:87], v[48:51]\n\t"
    "buffer_load_dwordx4 v18, s[40:43], s57 offen offset:1024 lds\n\t"
    "v_mfma_f32_16x16x32_bf16 v[48:51], a[0:3], a[68:71], v[48:51]\n\t"
    "v_mfma_f32_16x16x32_bf16 v[64:67], a[0:3], a[72:75], v[64:67]\n\t"
    "v_mfma_f32_16x16x32_bf16 v[64:67], a[16:19], a[88:91], v[64:67]\n\t"
    "v_mfma_f32_16x16x32_bf16 v[68:71], a[20:23], a[88:91], v[68:71]\n\t"
    "v_mfma_f32_16x16x32_bf16 v[68:71], a[4:7], a[72:75], v[68:71]\n\t"
    "buffer_load_dwordx4 v18, s[40:43], s58 offen offset:2048 lds\n\t"
    "v_mfma_f32_16x16x32_bf16 v[72:75], a[8:11], a[72:75], v[72:75]\n\t"
    "v_mfma_f32_16x16x32_bf16 v[72:75], a[24:27], a[88:91], v[72:75]\n\t"
    "v_mfma_f32_16x16x32_bf16 v[76:79], a[28:31], a[88:91], v[76:79]\n\t"
    "v_mfma_f32_16x16x32_bf16 v[76:79], a[12:15], a[72:75], v[76:79]\n\t"
    "v_mfma_f32_16x16x32_bf16 v[92:95], a[12:15], a[76:79], v[92:95]\n\t"
    "buffer_load_dwordx4 v18, s[40:43], s59 offen offset:3072 lds\n\t"
    "v_mfma_f32_16x16x32_bf16 v[92:95], a[28:31], a[92:95], v[92:95]\n\t"
    "s_mov_b64 s[40:41], s[82:83]\n\t"
    "s_add_u32 s69, s69, s64\n\t"
    "v_mfma_f32_16x16x32_bf16 v[88:91], a[24:27], a[92:95], v[88:91]\n\t"
    "s_sub_u32 s68, s68, 1\n\t"
    "v_mfma_f32_16x16x32_bf16 v[88:91], a[8:11], a[76:79], v[88:91]\n\t"
    "v_mfma_f32_16x16x32_bf16 v[84:87], a[4:7], a[76:79], v[84:87]\n\t"
    "v_mfma_f32_16x16x32_bf16 v[84:87], a[20:23], a[92:95], v[84:87]\n\t"
    "s_branch Lloopp%=\n\t"
    "Loutp%=:\n\t"
    "v_mfma_f32_16x16x32_bf16 v[80:83], a[48:51], a[92:95], v[80:83]\n\t"
    "s_waitcnt vmcnt(12) lgkmcnt(0)\n\t"
    "v_mfma_f32_16x16x32_bf16 v[80:83], a[32:35], a[76:79], v[80:83]\n\t"
    "s_barrier\n\t"
    "v_mfma_f32_16x16x32_bf16 v[96:99], a[32:35], a[96:99], v[96:99]\n\t"
    "ds_read_b128 a[192:195], v8 offset:0\n\t"
    "s_cmp_eq_u32 s73, 1\n\t"
    "v_mfma_f32_16x16x32_bf16 v[96:99], a[48:51], a[112:115], v[96:99]\n\t"
    "ds_read_b128 a[196:199], v8 offset:2048\n\t"
    "s_cselect_b32 s71, s76, s75\n\t"
    "v_mfma_f32_16x16x32_bf16 v[100:103], a[52:55], a[112:115], v[100:103]\n\t"
    "ds_read_b128 a[200:203], v9 offset:0\n\t"
    "v_add_u32 v160, s71, v8\n\t"
    "v_mfma_f32_16x16x32_bf16 v[100:103], a[36:39], a[96:99], v[100:103]\n\t"
    "ds_read_b128 a[204:207], v9 offset:2048\n\t"
    "v_add_u32 v161, s71, v9\n\t"
    "v_mfma_f32_16x16x32_bf16 v[104:107], a[40:43], a[96:99], v[104:107]\n\t"
    "ds_read_b64_tr_b16 a[128:129], v10 offset:0\n\t"
    "s_cmp_eq_u32 s74, 1\n\t"
    "v_mfma_f32_16x16x32_bf16 v[104:107], a[56:59], a[112:115], v[104:107]\n\t"
    "ds_read_b64_tr_b16 a[130:131], v10 offset:1024\n\t"
    "s_cselect_b32 s72, s78, s77\n\t"
    "v_mfma_f32_16x16x32_bf16 v[108:111], a[60:63], a[112:115], v[108:111]\n\t"
    "ds_read_b64_tr_b16 a[132:133], v11 offset:0\n\t"
    "v_add_u32 v164, s72, v10\n\t"
    "v_mfma_f32_16x16x32_bf16 v[108:111], a[44:47], a[96:99], v[108:111]\n\t"
    "ds_read_b64_tr_b16 a[134:135], v11 offset:1024\n\t"
    "v_add_u32 v165, s72, v11\n\t"
    "v_mfma_f32_16x16x32_bf16 v[124:127], a[44:47], a[100:103], v[124:127]\n\t"
    "ds_read_b64_tr_b16 a[136:137], v12 offset:0\n\t"
    "v_add_u32 v166, s72, v12\n\t"
    "v_mfma_f32_16x16x32_bf16 v[124:127], a[60:63], a[116:119], v[124:127]\n\t"
    "ds_read_b64_tr_b16 a[138:139], v12 offset:1024\n\t"
    "v_add_u32 v167, s72, v13\n\t"
    "v_mfma_f32_16x16x32_bf16 v[120:123], a[56:59], a[116:119], v[120:123]\n\t"
    "ds_read_b64_tr_b16 a[140:141], v13 offset:0\n\t"
    "s_bfe_u32 s24, %[b], 0x10001\n\t"
    "v_mfma_f32_16x16x32_bf16 v[120:123], a[40:43], a[100:103], v[120:123]\n\t"
    "ds_read_b64_tr_b16 a[142:143], v13 offset:1024\n\t"
    "s_mul_i32 s31, s24, 3670016\n\t"
    "v_mfma_f32_16x16x32_bf16 v[116:119], a[36:39], a[100:103], v[116:119]\n\t"
    "ds_read_b64_tr_b16 a[144:145], v10 offset:8192\n\t"
    "s_and_b32 s98, s26, 7\n\t"
    "v_mfma_f32_16x16x32_bf16 v[116:119], a[52:55], a[116:119], v[116:119]\n\t"
    "ds_read_b64_tr_b16 a[146:147], v10 offset:9216\n\t"
    "s_lshl_b32 s98, s98, 8\n\t"
    "v_mfma_f32_16x16x32_bf16 v[112:115], a[48:51], a[116:119], v[112:115]\n\t"
    "ds_read_b64_tr_b16 a[148:149], v11 offset:8192\n\t"
    "s_add_u32 s31, s31, s98\n\t"
    "v_mfma_f32_16x16x32_bf16 v[112:115], a[32:35], a[100:103], v[112:115]\n\t"
    "ds_read_b64_tr_b16 a[150:151], v11 offset:9216\n\t"
    "s_add_u32 s38, s16, s31\n\t"
    "v_mfma_f32_16x16x32_bf16 v[128:131], a[32:35], a[104:107], v[128:131]\n\t"
    "ds_read_b64_tr_b16 a[152:153], v12 offset:8192\n\t"
    "s_addc_u32 s39, s17, 0\n\t"
    "v_mfma_f32_16x16x32_bf16 v[128:131], a[48:51], a[120:123], v[128:131]\n\t"
    "ds_read_b64_tr_b16 a[154:155], v12 offset:9216\n\t"
    "s_and_b32 s98, %[b], 1\n\t"
    "v_mfma_f32_16x16x32_bf16 v[132:135], a[52:55], a[120:123], v[132:135]\n\t"
    "ds_read_b64_tr_b16 a[156:157], v13 offset:8192\n\t"
    "s_mul_i32 s87, s98, 3538944\n\t"
    "v_mfma_f32_16x16x32_bf16 v[132:135], a[36:39], a[104:107], v[132:135]\n\t"
    "ds_read_b64_tr_b16 a[158:159], v13 offset:9216\n\t"
    "s_mul_i32 s98, s98, 262144\n\t"
    "v_mfma_f32_16x16x32_bf16 v[136:139], a[40:43], a[104:107], v[136:139]\n\t"
    "s_sub_u32 s80, 131072, s98\n\t"
    "s_add_u32 s44, s38, s87\n\t"
    "v_mfma_f32_16x16x32_bf16 v[136:139], a[56:59], a[120:123], v[136:139]\n\t"
    "s_addc_u32 s45, s39, 0\n\t"
    "s_mov_b32 s46, 0x7fffffff\n\t"
    "v_mfma_f32_16x16x32_bf16 v[140:143], a[60:63], a[120:123], v[140:143]\n\t"
    "s_mov_b32 s47, 0x00020000\n\t"
    "v_mov_b32 v19, v2\n\t"
    "v_mfma_f32_16x16x32_bf16 v[140:143], a[44:47], a[104:107], v[140:143]\n\t"
    "v_xor_b32 v20, 128, v2\n\t"
    "s_add_u32 s70, s23, 98304\n\t"
    "v_mfma_f32_16x16x32_bf16 v[156:159], a[44:47], a[108:111], v[156:159]\n\t"
    "s_mov_b32 s77, 16384\n\t"
    "s_mov_b32 s60, 0\n\t"
    "v_mfma_f32_16x16x32_bf16 v[156:159], a[60:63], a[124:127], v[156:159]\n\t"
    "s_mov_b32 s61, 7168\n\t"
    "s_mov_b32 s62, 14336\n\t"
    "v_mfma_f32_16x16x32_bf16 v[152:155], a[56:59], a[124:127], v[152:155]\n\t"
    "s_mov_b32 s63, 21504\n\t"
    "v_mfma_f32_16x16x32_bf16 v[152:155], a[40:43], a[108:111], v[152:155]\n\t"
    "v_mfma_f32_16x16x32_bf16 v[148:151], a[36:39], a[108:111], v[148:151]\n\t"
    "v_mfma_f32_16x16x32_bf16 v[148:151], a[52:55], a[124:127], v[148:151]\n\t"
    "v_mfma_f32_16x16x32_bf16 v[144:147], a[48:51], a[124:127], v[144:147]\n\t"
    "v_mfma_f32_16x16x32_bf16 v[144:147], a[32:35], a[108:111], v[144:147]\n\t"
    "s_waitcnt vmcnt(0) lgkmcnt(0)\n\t"
    "s_barrier\n\t"
    "v_mfma_f32_16x16x32_bf16 v[32:35], a[128:131], a[192:195], v[32:35]\n\t"
    "ds_read_b128 a[208:211], v160 offset:0\n\t"
    "ds_read_b128 a[212:215], v160 offset:2048\n\t"
    "v_mfma_f32_16x16x32_bf16 v[32:35], a[144:147], a[200:203], v[32:35]\n\t"
    "ds_read_b128 a[216:219], v161 offset:0\n\t"
    "ds_read_b128 a[220:223], v161 offset:2048\n\t"
    "v_mfma_f32_16x16x32_bf16 v[36:39], a[132:135], a[192:195], v[36:39]\n\t"
    "ds_read_b64_tr_b16 a[160:161], v164 offset:0\n\t"
    "ds_read_b64_tr_b16 a[162:163], v164 offset:1024\n\t"
    "v_mfma_f32_16x16x32_bf16 v[36:39], a[148:151], a[200:203], v[36:39]\n\t"
    "ds_read_b64_tr_b16 a[164:165], v165 offset:0\n\t"
    "ds_read_b64_tr_b16 a[166:167], v165 offset:1024\n\t"
    "v_mfma_f32_16x16x32_bf16 v[40:43], a[136:139], a[192:195], v[40:43]\n\t"
    "ds_read_b64_tr_b16 a[168:169], v166 offset:0\n\t"
    "ds_read_b64_tr_b16 a[170:171], v166 offset:1024\n\t"
    "v_mfma_f32_16x16x32_bf16 v[40:43], a[152:155], a[200:203], v[40:43]\n\t"
    "ds_read_b64_tr_b16 a[172:173], v167 offset:0\n\t"
    "ds_read_b64_tr_b16 a[174:175], v167 offset:1024\n\t"
    "v_mfma_f32_16x16x32_bf16 v[44:47], a[140:143], a[192:195], v[44:47]\n\t"
    "ds_read_b64_tr_b16 a[176:177], v164 offset:8192\n\t"
    "ds_read_b64_tr_b16 a[178:179], v164 offset:9216\n\t"
    "v_mfma_f32_16x16x32_bf16 v[44:47], a[156:159], a[200:203], v[44:47]\n\t"
    "ds_read_b64_tr_b16 a[180:181], v165 offset:8192\n\t"
    "ds_read_b64_tr_b16 a[182:183], v165 offset:9216\n\t"
    "v_mfma_f32_16x16x32_bf16 v[60:63], a[140:143], a[196:199], v[60:63]\n\t"
    "ds_read_b64_tr_b16 a[184:185], v166 offset:8192\n\t"
    "ds_read_b64_tr_b16 a[186:187], v166 offset:9216\n\t"
    "v_mfma_f32_16x16x32_bf16 v[60:63], a[156:159], a[204:207], v[60:63]\n\t"
    "ds_read_b64_tr_b16 a[188:189], v167 offset:8192\n\t"
    "ds_read_b64_tr_b16 a[190:191], v167 offset:9216\n\t"
    "v_mfma_f32_16x16x32_bf16 v[56:59], a[136:139], a[196:199], v[56:59]\n\t"
    "v_mfma_f32_16x16x32_bf16 v[56:59], a[152:155], a[204:207], v[56:59]\n\t"
    "v_mfma_f32_16x16x32_bf16 v[52:55], a[132:135], a[196:199], v[52:55]\n\t"
    "v_mfma_f32_16x16x32_bf16 v[52:55], a[148:151], a[204:207], v[52:55]\n\t"
    "v_mfma_f32_16x16x32_bf16 v[48:51], a[128:131], a[196:199], v[48:51]\n\t"
    "v_mfma_f32_16x16x32_bf16 v[48:51], a[144:147], a[204:207], v[48:51]\n\t"
    "s_waitcnt lgkmcnt(0)\n\t"
    "v_mfma_f32_16x16x32_bf16 v[32:35], a[160:163], a[208:211], v[32:35]\n\t"
    "ds_read_b128 a[224:227], v8 offset:4096\n\t"
    "ds_read_b128 a[228:231], v8 offset:6144\n\t"
    "v_mfma_f32_16x16x32_bf16 v[32:35], a[176:179], a[216:219], v[32:35]\n\t"
    "ds_read_b128 a[232:235], v9 offset:4096\n\t"
    "ds_read_b128 a[236:239], v9 offset:6144\n\t"
    "v_mfma_f32_16x16x32_bf16 v[36:39], a[164:167], a[208:211], v[36:39]\n\t"
    "ds_read_b128 a[240:243], v160 offset:4096\n\t"
    "ds_read_b128 a[244:247], v160 offset:6144\n\t"
    "v_mfma_f32_16x16x32_bf16 v[36:39], a[180:183], a[216:219], v[36:39]\n\t"
    "ds_read_b128 a[248:251], v161 offset:4096\n\t"
    "ds_read_b128 a[252:255], v161 offset:6144\n\t"
    "s_mov_b32 m0, s70\n\t"
    "v_mfma_f32_16x16x32_bf16 v[40:43], a[168:171], a[208:211], v[40:43]\n\t"
    "buffer_load_dwordx4 v19, s[44:47], s60 offen offset:0 lds\n\t"
    "v_mfma_f32_16x16x32_bf16 v[40:43], a[184:187], a[216:219], v[40:43]\n\t"
    "buffer_load_dwordx4 v20, s[44:47], s61 offen offset:1024 lds\n\t"
    "v_mfma_f32_16x16x32_bf16 v[44:47], a[172:175], a[208:211], v[44:47]\n\t"
    "buffer_load_dwordx4 v19, s[44:47], s62 offen offset:2048 lds\n\t"
    "v_mfma_f32_16x16x32_bf16 v[44:47], a[188:191], a[216:219], v[44:47]\n\t"
    "v_mfma_f32_16x16x32_bf16 v[60:63], a[172:175], a[212:215], v[60:63]\n\t"
    "buffer_load_dwordx4 v20, s[44:47], s63 offen offset:3072 lds\n\t"
    "s_mul_i32 s44, s80, 1\n\t"
    "s_add_u32 s44, s44, s87\n\t"
    "s_add_u32 s44, s44, s38\n\t"
    "s_addc_u32 s45, s39, 0\n\t"
    "s_add_u32 s70, s70, s77\n\t"
    "s_mov_b32 m0, s70\n\t"
    "v_mfma_f32_16x16x32_bf16 v[60:63], a[188:191], a[220:223], v[60:63]\n\t"
    "buffer_load_dwordx4 v19, s[44:47], s60 offen offset:0 lds\n\t"
    "v_mfma_f32_16x16x32_bf16 v[56:59], a[168:171], a[212:215], v[56:59]\n\t"
    "buffer_load_dwordx4 v20, s[44:47], s61 offen offset:1024 lds\n\t"
    "v_mfma_f32_16x16x32_bf16 v[56:59], a[184:187], a[220:223], v[56:59]\n\t"
    "v_mfma_f32_16x16x32_bf16 v[52:55], a[164:167], a[212:215], v[52:55]\n\t"
    "buffer_load_dwordx4 v19, s[44:47], s62 offen offset:2048 lds\n\t"
    "v_mfma_f32_16x16x32_bf16 v[52:55], a[180:183], a[220:223], v[52:55]\n\t"
    "buffer_load_dwordx4 v20, s[44:47], s63 offen offset:3072 lds\n\t"
    "v_mfma_f32_16x16x32_bf16 v[48:51], a[160:163], a[212:215], v[48:51]\n\t"
    "s_mul_i32 s44, s80, 5\n\t"
    "s_add_u32 s44, s44, s87\n\t"
    "s_add_u32 s44, s44, s38\n\t"
    "v_mfma_f32_16x16x32_bf16 v[48:51], a[176:179], a[220:223], v[48:51]\n\t"
    "s_addc_u32 s45, s39, 0\n\t"
    "s_add_u32 s70, s70, s77\n\t"
    "s_waitcnt lgkmcnt(0)\n\t"
    "v_mfma_f32_16x16x32_bf16 v[64:67], a[128:131], a[224:227], v[64:67]\n\t"
    "ds_read_b128 a[192:195], v8 offset:8192\n\t"
    "s_mov_b32 s82, 0\n\t"
    "v_mfma_f32_16x16x32_bf16 v[64:67], a[144:147], a[232:235], v[64:67]\n\t"
    "v_cvt_pk_bf16_f32 v32, v32, v33\n\t"
    "v_cvt_pk_bf16_f32 v33, v34, v35\n\t"
    "v_mfma_f32_16x16x32_bf16 v[64:67], a[160:163], a[240:243], v[64:67]\n\t"
    "ds_read_b128 a[196:199], v8 offset:10240\n\t"
    "v_cvt_pk_bf16_f32 v34, v36, v37\n\t"
    "v_mfma_f32_16x16x32_bf16 v[64:67], a[176:179], a[248:251], v[64:67]\n\t"
    "v_cvt_pk_bf16_f32 v35, v38, v39\n\t"
    "v_xor_b32 v32, s35, v32\n\t"
    "v_mfma_f32_16x16x32_bf16 v[68:71], a[132:135], a[224:227], v[68:71]\n\t"
    "ds_read_b128 a[200:203], v9 offset:8192\n\t"
    "v_xor_b32 v33, s35, v33\n\t"
    "v_mfma_f32_16x16x32_bf16 v[68:71], a[148:151], a[232:235], v[68:71]\n\t"
    "v_xor_b32 v34, s35, v34\n\t"
    "v_xor_b32 v35, s35, v35\n\t"
    "v_mfma_f32_16x16x32_bf16 v[68:71], a[164:167], a[240:243], v[68:71]\n\t"
    "ds_read_b128 a[204:207], v9 offset:10240\n\t"
    "buffer_store_dwordx4 v[32:35], v23, s[48:51], s82 offen offset:0 sc1\n\t"
    "v_mfma_f32_16x16x32_bf16 v[68:71], a[180:183], a[248:251], v[68:71]\n\t"
    "s_mov_b32 s82, 131072\n\t"
    "v_cvt_pk_bf16_f32 v40, v40, v41\n\t"
    "v_mfma_f32_16x16x32_bf16 v[72:75], a[136:139], a[224:227], v[72:75]\n\t"
    "ds_read_b128 a[208:211], v160 offset:8192\n\t"
    "v_cvt_pk_bf16_f32 v41, v42, v43\n\t"
    "v_mfma_f32_16x16x32_bf16 v[72:75], a[152:155], a[232:235], v[72:75]\n\t"
    "v_cvt_pk_bf16_f32 v42, v44, v45\n\t"
    "v_cvt_pk_bf16_f32 v43, v46, v47\n\t"
    "v_mfma_f32_16x16x32_bf16 v[72:75], a[168:171], a[240:243], v[72:75]\n\t"
    "ds_read_b128 a[212:215], v160 offset:10240\n\t"
    "v_xor_b32 v40, s35, v40\n\t"
    "v_mfma_f32_16x16x32_bf16 v[72:75], a[184:187], a[248:251], v[72:75]\n\t"
    "v_xor_b32 v41, s35, v41\n\t"
    "v_xor_b32 v42, s35, v42\n\t"
    "v_mfma_f32_16x16x32_bf16 v[76:79], a[140:143], a[224:227], v[76:79]\n\t"
    "ds_read_b128 a[216:219], v161 offset:8192\n\t"
    "v_xor_b32 v43, s35, v43\n\t"
    "v_mfma_f32_16x16x32_bf16 v[76:79], a[156:159], a[232:235], v[76:79]\n\t"
    "buffer_store_dwordx4 v[40:43], v23, s[48:51], s82 offen offset:0 sc1\n\t"
    "s_mov_b32 s82, 0\n\t"
    "v_mfma_f32_16x16x32_bf16 v[76:79], a[172:175], a[240:243], v[76:79]\n\t"
    "ds_read_b128 a[220:223], v161 offset:10240\n\t"
    "v_cvt_pk_bf16_f32 v48, v48, v49\n\t"
    "v_mfma_f32_16x16x32_bf16 v[76:79], a[188:191], a[248:251], v[76:79]\n\t"
    "v_cvt_pk_bf16_f32 v49, v50, v51\n\t"
    "v_cvt_pk_bf16_f32 v50, v52, v53\n\t"
    "v_mfma_f32_16x16x32_bf16 v[92:95], a[140:143], a[228:231], v[92:95]\n\t"
    "v_cvt_pk_bf16_f32 v51, v54, v55\n\t"
    "v_xor_b32 v48, s35, v48\n\t"
    "v_mfma_f32_16x16x32_bf16 v[92:95], a[156:159], a[236:239], v[92:95]\n\t"
    "v_xor_b32 v49, s35, v49\n\t"
    "v_xor_b32 v50, s35, v50\n\t"
    "v_mfma_f32_16x16x32_bf16 v[92:95], a[172:175], a[244:247], v[92:95]\n\t"
    "v_xor_b32 v51, s35, v51\n\t"
    "buffer_store_dwordx4 v[48:51], v23, s[48:51], s82 offen offset:1024 sc1\n\t"
    "v_mfma_f32_16x16x32_bf16 v[92:95], a[188:191], a[252:255], v[92:95]\n\t"
    "s_mov_b32 s82, 131072\n\t"
    "v_cvt_pk_bf16_f32 v56, v56, v57\n\t"
    "v_mfma_f32_16x16x32_bf16 v[88:91], a[136:139], a[228:231], v[88:91]\n\t"
    "v_cvt_pk_bf16_f32 v57, v58, v59\n\t"
    "v_cvt_pk_bf16_f32 v58, v60, v61\n\t"
    "v_mfma_f32_16x16x32_bf16 v[88:91], a[152:155], a[236:239], v[88:91]\n\t"
    "v_cvt_pk_bf16_f32 v59, v62, v63\n\t"
    "v_xor_b32 v56, s35, v56\n\t"
    "v_mfma_f32_16x16x32_bf16 v[88:91], a[168:171], a[244:247], v[88:91]\n\t"
    "v_xor_b32 v57, s35, v57\n\t"
    "v_xor_b32 v58, s35, v58\n\t"
    "v_mfma_f32_16x16x32_bf16 v[88:91], a[184:187], a[252:255], v[88:91]\n\t"
    "v_xor_b32 v59, s35, v59\n\t"
    "buffer_store_dwordx4 v[56:59], v23, s[48:51], s82 offen offset:1024 sc1\n\t"
    "v_mfma_f32_16x16x32_bf16 v[84:87], a[132:135], a[228:231], v[84:87]\n\t"
    "v_mfma_f32_16x16x32_bf16 v[84:87], a[148:151], a[236:239], v[84:87]\n\t"
    "v_mfma_f32_16x16x32_bf16 v[84:87], a[164:167], a[244:247], v[84:87]\n\t"
    "v_mfma_f32_16x16x32_bf16 v[84:87], a[180:183], a[252:255], v[84:87]\n\t"
    "v_mfma_f32_16x16x32_bf16 v[80:83], a[128:131], a[228:231], v[80:83]\n\t"
    "v_mfma_f32_16x16x32_bf16 v[80:83], a[144:147], a[236:239], v[80:83]\n\t"
    "v_mfma_f32_16x16x32_bf16 v[80:83], a[160:163], a[244:247], v[80:83]\n\t"
    "v_mfma_f32_16x16x32_bf16 v[80:83], a[176:179], a[252:255], v[80:83]\n\t"
    "s_waitcnt lgkmcnt(0)\n\t"
    "v_mfma_f32_16x16x32_bf16 v[96:99], a[128:131], a[192:195], v[96:99]\n\t"
    "ds_read_b128 a[224:227], v8 offset:12288\n\t"
    "s_mov_b32 s82, 0\n\t"
    "v_mfma_f32_16x16x32_bf16 v[96:99], a[144:147], a[200:203], v[96:99]\n\t"
    "v_cvt_pk_bf16_f32 v64, v64, v65\n\t"
    "v_cvt_pk_bf16_f32 v65, v66, v67\n\t"
    "v_mfma_f32_16x16x32_bf16 v[96:99], a[160:163], a[208:211], v[96:99]\n\t"
    "ds_read_b128 a[228:231], v8 offset:14336\n\t"
    "v_cvt_pk_bf16_f32 v66, v68, v69\n\t"
    "v_mfma_f32_16x16x32_bf16 v[96:99], a[176:179], a[216:219], v[96:99]\n\t"
    "v_cvt_pk_bf16_f32 v67, v70, v71\n\t"
    "v_xor_b32 v64, s35, v64\n\t"
    "v_mfma_f32_16x16x32_bf16 v[100:103], a[132:135], a[192:195], v[100:103]\n\t"
    "ds_read_b128 a[232:235], v9 offset:12288\n\t"
    "v_xor_b32 v65, s35, v65\n\t"
    "v_mfma_f32_16x16x32_bf16 v[100:103], a[148:151], a[200:203], v[100:103]\n\t"
    "v_xor_b32 v66, s35, v66\n\t"
    "v_xor_b32 v67, s35, v67\n\t"
    "v_mfma_f32_16x16x32_bf16 v[100:103], a[164:167], a[208:211], v[100:103]\n\t"
    "ds_read_b128 a[236:239], v9 offset:14336\n\t"
    "buffer_store_dwordx4 v[64:67], v23, s[48:51], s82 offen offset:2048 sc1\n\t"
    "v_mfma_f32_16x16x32_bf16 v[100:103], a[180:183], a[216:219], v[100:103]\n\t"
    "s_mov_b32 s82, 131072\n\t"
    "v_cvt_pk_bf16_f32 v72, v72, v73\n\t"
    "v_mfma_f32_16x16x32_bf16 v[104:107], a[136:139], a[192:195], v[104:107]\n\t"
    "ds_read_b128 a[240:243], v160 offset:12288\n\t"
    "v_cvt_pk_bf16_f32 v73, v74, v75\n\t"
    "v_mfma_f32_16x16x32_bf16 v[104:107], a[152:155], a[200:203], v[104:107]\n\t"
    "v_cvt_pk_bf16_f32 v74, v76, v77\n\t"
    "v_cvt_pk_bf16_f32 v75, v78, v79\n\t"
    "v_mfma_f32_16x16x32_bf16 v[104:107], a[168:171], a[208:211], v[104:107]\n\t"
    "ds_read_b128 a[244:247], v160 offset:14336\n\t"
    "v_xor_b32 v72, s35, v72\n\t"
    "v_mfma_f32_16x16x32_bf16 v[104:107], a[184:187], a[216:219], v[104:107]\n\t"
    "v_xor_b32 v73, s35, v73\n\t"
    "v_xor_b32 v74, s35, v74\n\t"
    "v_mfma_f32_16x16x32_bf16 v[108:111], a[140:143], a[192:195], v[108:111]\n\t"
    "ds_read_b128 a[248:251], v161 offset:12288\n\t"
    "v_xor_b32 v75, s35, v75\n\t"
    "v_mfma_f32_16x16x32_bf16 v[108:111], a[156:159], a[200:203], v[108:111]\n\t"
    "buffer_store_dwordx4 v[72:75], v23, s[48:51], s82 offen offset:2048 sc1\n\t"
    "s_mov_b32 s82, 0\n\t"
    "v_mfma_f32_16x16x32_bf16 v[108:111], a[172:175], a[208:211], v[108:111]\n\t"
    "ds_read_b128 a[252:255], v161 offset:14336\n\t"
    "v_cvt_pk_bf16_f32 v80, v80, v81\n\t"
    "v_mfma_f32_16x16x32_bf16 v[108:111], a[188:191], a[216:219], v[108:111]\n\t"
    "v_cvt_pk_bf16_f32 v81, v82, v83\n\t"
    "v_cvt_pk_bf16_f32 v82, v84, v85\n\t"
    "v_mfma_f32_16x16x32_bf16 v[124:127], a[140:143], a[196:199], v[124:127]\n\t"
    "v_cvt_pk_bf16_f32 v83, v86, v87\n\t"
    "v_xor_b32 v80, s35, v80\n\t"
    "v_mfma_f32_16x16x32_bf16 v[124:127], a[156:159], a[204:207], v[124:127]\n\t"
    "v_xor_b32 v81, s35, v81\n\t"
    "v_xor_b32 v82, s35, v82\n\t"
    "v_mfma_f32_16x16x32_bf16 v[124:127], a[172:175], a[212:215], v[124:127]\n\t"
    "v_xor_b32 v83, s35, v83\n\t"
    "buffer_store_dwordx4 v[80:83], v23, s[48:51], s82 offen offset:3072 sc1\n\t"
    "v_mfma_f32_16x16x32_bf16 v[124:127], a[188:191], a[220:223], v[124:127]\n\t"
    "s_mov_b32 s82, 131072\n\t"
    "v_cvt_pk_bf16_f32 v88, v88, v89\n\t"
    "v_mfma_f32_16x16x32_bf16 v[120:123], a[136:139], a[196:199], v[120:123]\n\t"
    "v_cvt_pk_bf16_f32 v89, v90, v91\n\t"
    "v_cvt_pk_bf16_f32 v90, v92, v93\n\t"
    "v_mfma_f32_16x16x32_bf16 v[120:123], a[152:155], a[204:207], v[120:123]\n\t"
    "v_cvt_pk_bf16_f32 v91, v94, v95\n\t"
    "v_xor_b32 v88, s35, v88\n\t"
    "v_mfma_f32_16x16x32_bf16 v[120:123], a[168:171], a[212:215], v[120:123]\n\t"
    "v_xor_b32 v89, s35, v89\n\t"
    "v_xor_b32 v90, s35, v90\n\t"
    "v_mfma_f32_16x16x32_bf16 v[120:123], a[184:187], a[220:223], v[120:123]\n\t"
    "v_xor_b32 v91, s35, v91\n\t"
    "buffer_store_dwordx4 v[88:91], v23, s[48:51], s82 offen offset:3072 sc1\n\t"
    "v_mfma_f32_16x16x32_bf16 v[116:119], a[132:135], a[196:199], v[116:119]\n\t"
    "v_mfma_f32_16x16x32_bf16 v[116:119], a[148:151], a[204:207], v[116:119]\n\t"
    "v_mfma_f32_16x16x32_bf16 v[116:119], a[164:167], a[212:215], v[116:119]\n\t"
    "v_mfma_f32_16x16x32_bf16 v[116:119], a[180:183], a[220:223], v[116:119]\n\t"
    "v_mfma_f32_16x16x32_bf16 v[112:115], a[128:131], a[196:199], v[112:115]\n\t"
    "v_mfma_f32_16x16x32_bf16 v[112:115], a[144:147], a[204:207], v[112:115]\n\t"
    "v_mfma_f32_16x16x32_bf16 v[112:115], a[160:163], a[212:215], v[112:115]\n\t"
    "v_mfma_f32_16x16x32_bf16 v[112:115], a[176:179], a[220:223], v[112:115]\n\t"
    "s_waitcnt lgkmcnt(0)\n\t"
    "s_barrier\n\t"
    "v_mfma_f32_16x16x32_bf16 v[128:131], a[128:131], a[224:227], v[128:131]\n\t"
    "s_mov_b32 s82, 4096\n\t"
    "v_cvt_pk_bf16_f32 v96, v96, v97\n\t"
    "v_mfma_f32_16x16x32_bf16 v[128:131], a[144:147], a[232:235], v[128:131]\n\t"
    "v_cvt_pk_bf16_f32 v97, v98, v99\n\t"
    "v_cvt_pk_bf16_f32 v98, v100, v101\n\t"
    "v_mfma_f32_16x16x32_bf16 v[128:131], a[160:163], a[240:243], v[128:131]\n\t"
    "v_cvt_pk_bf16_f32 v99, v102, v103\n\t"
    "v_xor_b32 v96, s35, v96\n\t"
    "v_mfma_f32_16x16x32_bf16 v[128:131], a[176:179], a[248:251], v[128:131]\n\t"
    "v_xor_b32 v97, s35, v97\n\t"
    "v_xor_b32 v98, s35, v98\n\t"
    "v_mfma_f32_16x16x32_bf16 v[132:135], a[132:135], a[224:227], v[132:135]\n\t"
    "v_xor_b32 v99, s35, v99\n\t"
    "buffer_store_dwordx4 v[96:99], v23, s[48:51], s82 offen offset:0 sc1\n\t"
    "v_mfma_f32_16x16x32_bf16 v[132:135], a[148:151], a[232:235], v[132:135]\n\t"
    "s_mov_b32 s82, 135168\n\t"
    "v_cvt_pk_bf16_f32 v104, v104, v105\n\t"
    "v_mfma_f32_16x16x32_bf16 v[132:135], a[164:167], a[240:243], v[132:135]\n\t"
    "v_cvt_pk_bf16_f32 v105, v106, v107\n\t"
    "v_cvt_pk_bf16_f32 v106, v108, v109\n\t"
    "v_mfma_f32_16x16x32_bf16 v[132:135], a[180:183], a[248:251], v[132:135]\n\t"
    "v_cvt_pk_bf16_f32 v107, v110, v111\n\t"
    "v_xor_b32 v104, s35, v104\n\t"
    "v_mfma_f32_16x16x32_bf16 v[136:139], a[136:139], a[224:227], v[136:139]\n\t"
    "v_xor_b32 v105, s35, v105\n\t"
    "v_xor_b32 v106, s35, v106\n\t"
    "v_mfma_f32_16x16x32_bf16 v[136:139], a[152:155], a[232:235], v[136:139]\n\t"
    "v_xor_b32 v107, s35, v107\n\t"
    "buffer_store_dwordx4 v[104:107], v23, s[48:51], s82 offen offset:0 sc1\n\t"
    "v_mfma_f32_16x16x32_bf16 v[136:139], a[168:171], a[240:243], v[136:139]\n\t"
    "s_mov_b32 s82, 4096\n\t"
    "v_cvt_pk_bf16_f32 v112, v112, v113\n\t"
    "v_mfma_f32_16x16x32_bf16 v[136:139], a[184:187], a[248:251], v[136:139]\n\t"
    "v_cvt_pk_bf16_f32 v113, v114, v115\n\t"
    "v_cvt_pk_bf16_f32 v114, v116, v117\n\t"
    "v_mfma_f32_16x16x32_bf16 v[140:143], a[140:143], a[224:227], v[140:143]\n\t"
    "v_cvt_pk_bf16_f32 v115, v118, v119\n\t"
    "v_xor_b32 v112, s35, v112\n\t"
    "v_mfma_f32_16x16x32_bf16 v[140:143], a[156:159], a[232:235], v[140:143]\n\t"
    "v_xor_b32 v113, s35, v113\n\t"
    "v_xor_b32 v114, s35, v114\n\t"
    "v_mfma_f32_16x16x32_bf16 v[140:143], a[172:175], a[240:243], v[140:143]\n\t"
    "v_xor_b32 v115, s35, v115\n\t"
    "buffer_store_dwordx4 v[112:115], v23, s[48:51], s82 offen offset:1024 sc1\n\t"
    "v_mfma_f32_16x16x32_bf16 v[140:143], a[188:191], a[248:251], v[140:143]\n\t"
    "s_mov_b32 s82, 135168\n\t"
    "v_cvt_pk_bf16_f32 v120, v120, v121\n\t"
    "v_mfma_f32_16x16x32_bf16 v[156:159], a[140:143], a[228:231], v[156:159]\n\t"
    "v_cvt_pk_bf16_f32 v121, v122, v123\n\t"
    "v_cvt_pk_bf16_f32 v122, v124, v125\n\t"
    "v_mfma_f32_16x16x32_bf16 v[156:159], a[156:159], a[236:239], v[156:159]\n\t"
    "v_cvt_pk_bf16_f32 v123, v126, v127\n\t"
    "v_xor_b32 v120, s35, v120\n\t"
    "v_mfma_f32_16x16x32_bf16 v[156:159], a[172:175], a[244:247], v[156:159]\n\t"
    "v_xor_b32 v121, s35, v121\n\t"
    "v_xor_b32 v122, s35, v122\n\t"
    "v_mfma_f32_16x16x32_bf16 v[156:159], a[188:191], a[252:255], v[156:159]\n\t"
    "v_xor_b32 v123, s35, v123\n\t"
    "buffer_store_dwordx4 v[120:123], v23, s[48:51], s82 offen offset:1024 sc1\n\t"
    "v_mfma_f32_16x16x32_bf16 v[152:155], a[136:139], a[228:231], v[152:155]\n\t"
    "v_mfma_f32_16x16x32_bf16 v[152:155], a[152:155], a[236:239], v[152:155]\n\t"
    "v_mfma_f32_16x16x32_bf16 v[152:155], a[168:171], a[244:247], v[152:155]\n\t"
    "v_mfma_f32_16x16x32_bf16 v[152:155], a[184:187], a[252:255], v[152:155]\n\t"
    "v_mfma_f32_16x16x32_bf16 v[148:151], a[132:135], a[228:231], v[148:151]\n\t"
    "v_mfma_f32_16x16x32_bf16 v[148:151], a[148:151], a[236:239], v[148:151]\n\t"
    "v_mfma_f32_16x16x32_bf16 v[148:151], a[164:167], a[244:247], v[148:151]\n\t"
    "v_mfma_f32_16x16x32_bf16 v[148:151], a[180:183], a[252:255], v[148:151]\n\t"
    "v_mfma_f32_16x16x32_bf16 v[144:147], a[128:131], a[228:231], v[144:147]\n\t"
    "v_mfma_f32_16x16x32_bf16 v[144:147], a[144:147], a[236:239], v[144:147]\n\t"
    "v_mfma_f32_16x16x32_bf16 v[144:147], a[160:163], a[244:247], v[144:147]\n\t"
    "v_mfma_f32_16x16x32_bf16 v[144:147], a[176:179], a[252:255], v[144:147]\n\t"
    "s_mov_b32 s82, 4096\n\t"
    "v_cvt_pk_bf16_f32 v128, v128, v129\n\t"
    "v_cvt_pk_bf16_f32 v129, v130, v131\n\t"
    "v_cvt_pk_bf16_f32 v130, v132, v133\n\t"
    "v_cvt_pk_bf16_f32 v131, v134, v135\n\t"
    "v_xor_b32 v128, s35, v128\n\t"
    "v_xor_b32 v129, s35, v129\n\t"
    "v_xor_b32 v130, s35, v130\n\t"
    "v_xor_b32 v131, s35, v131\n\t"
    "buffer_store_dwordx4 v[128:131], v23, s[48:51], s82 offen offset:2048 sc1\n\t"
    "s_mov_b32 s82, 135168\n\t"
    "v_cvt_pk_bf16_f32 v136, v136, v137\n\t"
    "v_cvt_pk_bf16_f32 v137, v138, v139\n\t"
    "v_cvt_pk_bf16_f32 v138, v140, v141\n\t"
    "v_cvt_pk_bf16_f32 v139, v142, v143\n\t"
    "v_xor_b32 v136, s35, v136\n\t"
    "v_xor_b32 v137, s35, v137\n\t"
    "v_xor_b32 v138, s35, v138\n\t"
    "v_xor_b32 v139, s35, v139\n\t"
    "buffer_store_dwordx4 v[136:139], v23, s[48:51], s82 offen offset:2048 sc1\n\t"
    "s_mov_b32 s82, 4096\n\t"
    "v_cvt_pk_bf16_f32 v144, v144, v145\n\t"
    "v_cvt_pk_bf16_f32 v145, v146, v147\n\t"
    "v_cvt_pk_bf16_f32 v146, v148, v149\n\t"
    "v_cvt_pk_bf16_f32 v147, v150, v151\n\t"
    "v_xor_b32 v144, s35, v144\n\t"
    "v_xor_b32 v145, s35, v145\n\t"
    "v_xor_b32 v146, s35, v146\n\t"
    "v_xor_b32 v147, s35, v147\n\t"
    "buffer_store_dwordx4 v[144:147], v23, s[48:51], s82 offen offset:3072 sc1\n\t"
    "s_mov_b32 s82, 135168\n\t"
    "v_cvt_pk_bf16_f32 v152, v152, v153\n\t"
    "v_cvt_pk_bf16_f32 v153, v154, v155\n\t"
    "v_cvt_pk_bf16_f32 v154, v156, v157\n\t"
    "v_cvt_pk_bf16_f32 v155, v158, v159\n\t"
    "v_xor_b32 v152, s35, v152\n\t"
    "v_xor_b32 v153, s35, v153\n\t"
    "v_xor_b32 v154, s35, v154\n\t"
    "v_xor_b32 v155, s35, v155\n\t"
    "buffer_store_dwordx4 v[152:155], v23, s[48:51], s82 offen offset:3072 sc1\n\t"
    "Ls2%=:\n\t"
    "s_and_b32 s35, %[b], 3\n\t"
    "s_and_b32 s85, %[b], 4\n\t"
    "s_lshr_b32 s31, s26, 3\n\t"
    "s_add_u32 s85, s85, s31\n\t"
    "s_and_b32 s86, s26, 7\n\t"
    "s_mul_i32 s31, s24, 11534336\n\t"
    "s_lshl_b32 s98, s85, 14\n\t"
    "s_add_u32 s31, s31, s98\n\t"
    "s_add_u32 s31, s31, 4194304\n\t"
    "s_add_u32 s58, s18, s31\n\t"
    "s_addc_u32 s59, s19, 0\n\t"
    "s_and_b32 s98, %[b], 1\n\t"
    "s_mul_i32 s55, s98, 7077888\n\t"
    "s_mul_i32 s98, s98, 524288\n\t"
    "s_sub_u32 s56, 262144, s98\n\t"
    "s_add_u32 s40, s58, s55\n\t"
    "s_addc_u32 s41, s59, 0\n\t"
    "s_mov_b32 s42, 0x7fffffff\n\t"
    "s_mov_b32 s43, 0x00020000\n\t"
    "s_mov_b32 s52, 0\n\t"
    "s_mov_b32 s53, 131072\n\t"
    "v_lshl_add_u32 v18, v1, 4, s23\n\t"
    "s_mov_b32 s69, s23\n\t"
    "s_mov_b32 s75, 32768\n\t"
    "s_and_b32 s31, s85, 6\n\t"
    "s_add_u32 s31, s31, s24\n\t"
    "s_lshl_b32 s31, s31, 1\n\t"
    "s_and_b32 s98, s85, 1\n\t"
    "s_add_u32 s31, s31, s98\n\t"
    "s_lshl_b32 s31, s31, 8\n\t"
    "s_add_u32 s31, s31, 16384\n\t"
    "v_and_b32 v3, 7, v0\n\t"
    "v_lshl_add_u32 v29, v3, 2, s31\n\t"
    "s_and_b32 s98, %[b], 1\n\t"
    "s_lshl_b32 s98, s98, 4\n\t"
    "s_add_u32 s31, s31, s98\n\t"
    "v_and_b32 v3, 3, v0\n\t"
    "v_lshl_add_u32 v27, v3, 2, s31\n\t"
    "global_load_dword v28, v27, s[28:29] offset:192 sc1\n\t"
    "s_lshr_b32 s33, s22, 1\n\t"
    "s_and_b32 s34, s22, 1\n\t"
    "s_and_b32 s98, %[b], 1\n\t"
    "s_mov_b32 s66, 103198983\n\t"
    "s_mov_b32 s67, 146893385\n\t"
    "s_cmp_eq_u32 s24, 0\n\t"
    "s_cselect_b64 s[36:37], s[10:11], s[12:13]\n\t"
    "s_lshl_b32 s31, s86, 18\n\t"
    "s_lshl_b32 s98, s98, 10\n\t"
    "s_add_u32 s31, s31, s98\n\t"
    "s_add_u32 s36, s36, s31\n\t"
    "s_addc_u32 s37, s37, 0\n\t"
    "s_lshl_b32 s31, s85, 19\n\t"
    "s_lshl_b32 s98, s86, 8\n\t"
    "s_add_u32 s31, s31, s98\n\t"
    "s_add_u32 s92, s4, s31\n\t"
    "s_addc_u32 s93, s5, 0\n\t"
    "s_mov_b32 s94, 0x7fffffff\n\t"
    "s_mov_b32 s95, 0x00020000\n\t"
    "s_add_u32 s48, s20, s31\n\t"
    "s_addc_u32 s49, s21, 0\n\t"
    "s_mov_b32 s50, 0x7fffffff\n\t"
    "s_mov_b32 s51, 0x00020000\n\t"
    "s_lshl_b32 s31, s85, 3\n\t"
    "s_add_u32 s31, s31, s86\n\t"
    "s_mul_i32 s31, s31, 196608\n\t"
    "s_add_u32 s31, s31, 29360128\n\t"
    "s_add_u32 s88, s18, s31\n\t"
    "s_addc_u32 s89, s19, 0\n\t"
    "s_mov_b32 s90, 0x7fffffff\n\t"
    "s_mov_b32 s91, 0x00020000\n\t"
    "s_and_b32 s31, %[b], 1\n\t"
    "s_mul_i32 s31, s31, 2\n\t"
    "s_sub_u32 s68, 23, s31\n\t"
    "s_sub_u32 s57, s68, 6\n\t"
    "s_add_u32 s79, s57, 3\n\t"
    "s_mov_b32 s76, -65536\n\t"
    "s_mov_b32 s71, -65536\n\t"
    "s_mov_b32 s73, 3\n\t"
    "s_mov_b32 s78, -32768\n\t"
    "s_mov_b32 s72, -32768\n\t"
    "s_mov_b32 s74, 3\n\t"
    "v_lshrrev_b32 v6, 4, v1\n\t"
    "v_and_b32 v7, 15, v0\n\t"
    "v_lshrrev_b32 v3, 3, v1\n\t"
    "v_lshl_add_u32 v3, s22, 5, v3\n\t"
    "v_lshlrev_b32 v3, 11, v3\n\t"
    "v_and_b32 v4, 7, v0\n\t"
    "v_xor_b32 v4, v4, v6\n\t"
    "v_lshl_add_u32 v164, v4, 4, v3\n\t"
    "s_lshl_b32 s31, s33, 13\n\t"
    "s_lshl_b32 s98, s35, 11\n\t"
    "s_add_u32 s31, s31, s98\n\t"
    "v_lshl_add_u32 v8, v1, 4, s31\n\t"
    "v_xor_b32 v9, 2048, v8\n\t"
    "v_xor_b32 v10, 4096, v8\n\t"
    "v_xor_b32 v11, 6144, v8\n\t"
    "v_lshrrev_b32 v3, 2, v1\n\t"
    "v_and_b32 v4, 7, v3\n\t"
    "v_lshlrev_b32 v3, 8, v3\n\t"
    "v_lshl_add_u32 v3, v4, 5, v3\n\t"
    "v_and_b32 v4, 3, v0\n\t"
    "v_lshl_add_u32 v3, v4, 3, v3\n\t"
    "s_lshl_b32 s31, s34, 7\n\t"
    "v_xor_b32 v5, s31, v3\n\t"
    "v_add_u32 v12, 98304, v5\n\t"
    "v_xor_b32 v3, v6, v7\n\t"
    "v_lshrrev_b32 v3, 1, v3\n\t"
    "v_lshlrev_b32 v3, 4, v3\n\t"
    "v_lshl_add_u32 v3, v7, 7, v3\n\t"
    "v_and_b32 v4, 1, v6\n\t"
    "v_lshl_add_u32 v3, v4, 3, v3\n\t"
    "s_lshl_b32 s31, s34, 13\n\t"
    "v_add_u32 v3, s31, v3\n\t"
    "v_sub_u32 v160, v3, v5\n\t"
    "s_lshl_b32 s31, s35, 14\n\t"
    "s_add_u32 s31, s31, s23\n\t"
    "v_lshl_add_u32 v24, v1, 4, s31\n\t"
    "s_lshl_b32 s31, s33, 18\n\t"
    "s_lshl_b32 s98, s35, 16\n\t"
    "s_add_u32 s31, s31, s98\n\t"
    "s_lshl_b32 s98, s34, 7\n\t"
    "s_add_u32 s31, s31, s98\n\t"
    "v_lshl_add_u32 v3, v7, 11, s31\n\t"
    "v_and_b32 v4, 1, v6\n\t"
    "v_lshl_add_u32 v3, v4, 5, v3\n\t"
    "v_lshrrev_b32 v4, 1, v6\n\t"
    "v_lshl_add_u32 v25, v4, 4, v3\n\t"
    "v_xor_b32 v13, 32, v12\n\t"
    "v_xor_b32 v14, 64, v12\n\t"
    "v_xor_b32 v15, 96, v12\n\t"
    "s_waitcnt vmcnt(0)\n\t"
    "v_cmp_ne_u32 vcc, s27, v28\n\t"
    "s_nop 4\n\t"
    "s_cbranch_vccz Lsdh%=\n\t"
    "s_mov_b32 s84, 0x20000\n\t"
    "Lsph%=:\n\t"
    "global_load_dword v28, v27, s[28:29] offset:192 sc1\n\t"
    "s_waitcnt vmcnt(0)\n\t"
    "v_cmp_ne_u32 vcc, s27, v28\n\t"
    "s_sub_u32 s84, s84, 1\n\t"
    "s_cbranch_scc1 Lsdh%=\n\t"
    "s_nop 2\n\t"
    "s_cbranch_vccz Lsdh%=\n\t"
    "s_sleep 2\n\t"
    "s_branch Lsph%=\n\t"
    "Lsdh%=:\n\t"
    "s_mov_b32 m0, s69\n\t"
    "s_nop 0\n\t"
    "buffer_load_dwordx4 v18, s[40:43], s52 offen offset:0 sc1 lds\n\t"
    "buffer_load_dwordx4 v18, s[40:43], s52 offen offset:1024 sc1 lds\n\t"
    "buffer_load_dwordx4 v18, s[40:43], s52 offen offset:2048 sc1 lds\n\t"
    "buffer_load_dwordx4 v18, s[40:43], s52 offen offset:3072 sc1 lds\n\t"
    "s_bitset1_b32 m0, 14\n\t"
    "s_nop 0\n\t"
    "buffer_load_dwordx4 v18, s[40:43], s53 offen offset:0 sc1 lds\n\t"
    "buffer_load_dwordx4 v18, s[40:43], s53 offen offset:1024 sc1 lds\n\t"
    "buffer_load_dwordx4 v18, s[40:43], s53 offen offset:2048 sc1 lds\n\t"
    "buffer_load_dwordx4 v18, s[40:43], s53 offen offset:3072 sc1 lds\n\t"
    "s_add_u32 s55, s55, s56\n\t"
    "s_add_u32 s40, s58, s55\n\t"
    "s_addc_u32 s41, s59, 0\n\t"
    "s_add_u32 s69, s69, s75\n\t"
    "s_mov_b32 m0, s69\n\t"
    "s_nop 0\n\t"
    "buffer_load_dwordx4 v18, s[40:43], s52 offen offset:0 sc1 lds\n\t"
    "buffer_load_dwordx4 v18, s[40:43], s52 offen offset:1024 sc1 lds\n\t"
    "buffer_load_dwordx4 v18, s[40:43], s52 offen offset:2048 sc1 lds\n\t"
    "buffer_load_dwordx4 v18, s[40:43], s52 offen offset:3072 sc1 lds\n\t"
    "s_bitset1_b32 m0, 14\n\t"
    "s_nop 0\n\t"
    "buffer_load_dwordx4 v18, s[40:43], s53 offen offset:0 sc1 lds\n\t"
    "buffer_load_dwordx4 v18, s[40:43], s53 offen offset:1024 sc1 lds\n\t"
    "buffer_load_dwordx4 v18, s[40:43], s53 offen offset:2048 sc1 lds\n\t"
    "buffer_load_dwordx4 v18, s[40:43], s53 offen offset:3072 sc1 lds\n\t"
    "s_add_u32 s55, s55, s56\n\t"
    "s_add_u32 s40, s58, s55\n\t"
    "s_addc_u32 s41, s59, 0\n\t"
    "s_add_u32 s69, s69, s75\n\t"
    "v_mov_b64 v[96:97], 0\n\t"
    "v_mov_b64 v[98:99], 0\n\t"
    "v_mov_b64 v[100:101], 0\n\t"
    "v_mov_b64 v[102:103], 0\n\t"
    "v_mov_b64 v[104:105], 0\n\t"
    "v_mov_b64 v[106:107], 0\n\t"
    "v_mov_b64 v[108:109], 0\n\t"
    "v_mov_b64 v[110:111], 0\n\t"
    "v_mov_b64 v[124:125], 0\n\t"
    "v_mov_b64 v[126:127], 0\n\t"
    "v_mov_b64 v[120:121], 0\n\t"
    "v_mov_b64 v[122:123], 0\n\t"
    "v_mov_b64 v[116:117], 0\n\t"
    "v_mov_b64 v[118:119], 0\n\t"
    "v_mov_b64 v[112:113], 0\n\t"
    "v_mov_b64 v[114:115], 0\n\t"
    "v_mov_b64 v[128:129], 0\n\t"
    "v_mov_b64 v[130:131], 0\n\t"
    "v_mov_b64 v[132:133], 0\n\t"
    "v_mov_b64 v[134:135], 0\n\t"
    "v_mov_b64 v[136:137], 0\n\t"
    "v_mov_b64 v[138:139], 0\n\t"
    "v_mov_b64 v[140:141], 0\n\t"
    "v_mov_b64 v[142:143], 0\n\t"
    "v_mov_b64 v[156:157], 0\n\t"
    "v_mov_b64 v[158:159], 0\n\t"
    "v_mov_b64 v[152:153], 0\n\t"
    "v_mov_b64 v[154:155], 0\n\t"
    "v_mov_b64 v[148:149], 0\n\t"
    "v_mov_b64 v[150:151], 0\n\t"
    "v_mov_b64 v[144:145], 0\n\t"
    "v_mov_b64 v[146:147], 0\n\t"
    "v_mov_b64 v[80:81], 0\n\t"
    "v_mov_b64 v[82:83], 0\n\t"
    "s_waitcnt vmcnt(8)\n\t"
    "s_barrier\n\t"
    "ds_read_b64_tr_b16 a[0:1], v12 offset:0\n\t"
    "ds_read_b64_tr_b16 a[2:3], v12 offset:4096\n\t"
    "ds_read_b64_tr_b16 a[4:5], v13 offset:0\n\t"
    "ds_read_b64_tr_b16 a[6:7], v13 offset:4096\n\t"
    "ds_read_b64_tr_b16 a[8:9], v14 offset:0\n\t"
    "ds_read_b64_tr_b16 a[10:11], v14 offset:4096\n\t"
    "ds_read_b64_tr_b16 a[12:13], v15 offset:0\n\t"
    "ds_read_b64_tr_b16 a[14:15], v15 offset:4096\n\t"
    "ds_read_b64_tr_b16 a[16:17], v12 offset:8192\n\t"
    "ds_read_b64_tr_b16 a[18:19], v12 offset:12288\n\t"
    "ds_read_b64_tr_b16 a[20:21], v13 offset:8192\n\t"
    "ds_read_b64_tr_b16 a[22:23], v13 offset:12288\n\t"
    "ds_read_b64_tr_b16 a[24:25], v14 offset:8192\n\t"
    "ds_read_b64_tr_b16 a[26:27], v14 offset:12288\n\t"
    "ds_read_b64_tr_b16 a[28:29], v15 offset:8192\n\t"
    "ds_read_b64_tr_b16 a[30:31], v15 offset:12288\n\t"
    "ds_read_b128 a[64:67], v8 offset:0\n\t"
    "ds_read_b128 a[80:83], v8 offset:16384\n\t"
    "ds_read_b128 a[68:71], v8 offset:1024\n\t"
    "ds_read_b128 a[84:87], v8 offset:17408\n\t"
    "ds_read_b128 a[72:75], v9 offset:0\n\t"
    "ds_read_b128 a[88:91], v9 offset:16384\n\t"
    "ds_read_b128 a[76:79], v9 offset:1024\n\t"
    "ds_read_b128 a[92:95], v9 offset:17408\n\t"
    "s_cmp_lg_u32 s22, 0\n\t"
    "s_cbranch_scc1 Lop%=\n\t"
    "v_mov_b32 v3, s30\n\t"
    "v_mov_b32 v4, s27\n\t"
    "s_mov_b64 exec, 1\n\t"
    "global_store_dword v3, v4, s[28:29] sc1\n\t"
    "s_mov_b64 exec, -1\n\t"
    "Lop%=:\n\t"
    "s_waitcnt lgkmcnt(0)\n\t"
    "v_mfma_f32_16x16x32_bf16 v[32:35], a[0:3], a[64:67], 0\n\t"
    "ds_read_b128 a[96:99], v10 offset:0\n\t"
    "s_mov_b32 m0, s70\n\t"
    "v_mfma_f32_16x16x32_bf16 v[32:35], a[16:19], a[80:83], v[32:35]\n\t"
    "ds_read_b128 a[112:115], v10 offset:16384\n\t"
    "buffer_load_dwordx4 v19, s[44:47], s60 offen offset:0 lds\n\t"
    "v_mfma_f32_16x16x32_bf16 v[36:39], a[20:23], a[80:83], 0\n\t"
    "ds_read_b128 a[100:103], v10 offset:1024\n\t"
    "buffer_load_dwordx4 v20, s[44:47], s61 offen offset:1024 lds\n\t"
    "v_mfma_f32_16x16x32_bf16 v[36:39], a[4:7], a[64:67], v[36:39]\n\t"
    "ds_read_b128 a[116:119], v10 offset:17408\n\t"
    "v_mfma_f32_16x16x32_bf16 v[40:43], a[8:11], a[64:67], 0\n\t"
    "ds_read_b128 a[104:107], v11 offset:0\n\t"
    "buffer_load_dwordx4 v19, s[44:47], s62 offen offset:2048 lds\n\t"
    "v_mfma_f32_16x16x32_bf16 v[40:43], a[24:27], a[80:83], v[40:43]\n\t"
    "ds_read_b128 a[120:123], v11 offset:16384\n\t"
    "buffer_load_dwordx4 v20, s[44:47], s63 offen offset:3072 lds\n\t"
    "v_mfma_f32_16x16x32_bf16 v[44:47], a[28:31], a[80:83], 0\n\t"
    "ds_read_b128 a[108:111], v11 offset:1024\n\t"
    "v_mfma_f32_16x16x32_bf16 v[44:47], a[12:15], a[64:67], v[44:47]\n\t"
    "s_mul_i32 s44, s80, 6\n\t"
    "s_add_u32 s44, s44, s87\n\t"
    "s_add_u32 s44, s44, s38\n\t"
    "v_mfma_f32_16x16x32_bf16 v[60:63], a[12:15], a[68:71], 0\n\t"
    "s_addc_u32 s45, s39, 0\n\t"
    "s_add_u32 s70, s70, s72\n\t"
    "s_mov_b32 m0, s69\n\t"
    "v_mfma_f32_16x16x32_bf16 v[60:63], a[28:31], a[84:87], v[60:63]\n\t"
    "ds_read_b128 a[124:127], v11 offset:17408\n\t"
    "buffer_load_dwordx4 v18, s[40:43], s52 offen offset:0 sc1 lds\n\t"
    "v_mfma_f32_16x16x32_bf16 v[56:59], a[24:27], a[84:87], 0\n\t"
    "buffer_load_dwordx4 v18, s[40:43], s52 offen offset:1024 sc1 lds\n\t"
    "v_mfma_f32_16x16x32_bf16 v[56:59], a[8:11], a[68:71], v[56:59]\n\t"
    "v_mfma_f32_16x16x32_bf16 v[52:55], a[4:7], a[68:71], 0\n\t"
    "buffer_load_dwordx4 v18, s[40:43], s52 offen offset:2048 sc1 lds\n\t"
    "v_mfma_f32_16x16x32_bf16 v[52:55], a[20:23], a[84:87], v[52:55]\n\t"
    "buffer_load_dwordx4 v18, s[40:43], s52 offen offset:3072 sc1 lds\n\t"
    "v_mfma_f32_16x16x32_bf16 v[48:51], a[16:19], a[84:87], 0\n\t"
    "s_bitset1_b32 m0, 14\n\t"
    "v_mfma_f32_16x16x32_bf16 v[48:51], a[0:3], a[68:71], v[48:51]\n\t"
    "buffer_load_dwordx4 v18, s[40:43], s53 offen offset:0 sc1 lds\n\t"
    "v_mfma_f32_16x16x32_bf16 v[64:67], a[0:3], a[72:75], 0\n\t"
    "buffer_load_dwordx4 v18, s[40:43], s53 offen offset:1024 sc1 lds\n\t"
    "v_mfma_f32_16x16x32_bf16 v[64:67], a[16:19], a[88:91], v[64:67]\n\t"
    "v_mfma_f32_16x16x32_bf16 v[68:71], a[20:23], a[88:91], 0\n\t"
    "buffer_load_dwordx4 v18, s[40:43], s53 offen offset:2048 sc1 lds\n\t"
    "v_mfma_f32_16x16x32_bf16 v[68:71], a[4:7], a[72:75], v[68:71]\n\t"
    "buffer_load_dwordx4 v18, s[40:43], s53 offen offset:3072 sc1 lds\n\t"
    "v_mfma_f32_16x16x32_bf16 v[72:75], a[8:11], a[72:75], 0\n\t"
    "s_add_u32 s55, s55, s56\n\t"
    "s_add_u32 s40, s58, s55\n\t"
    "s_addc_u32 s41, s59, 0\n\t"
    "v_mfma_f32_16x16x32_bf16 v[72:75], a[24:27], a[88:91], v[72:75]\n\t"
    "s_add_u32 s69, s69, s71\n\t"
    "s_sub_u32 s73, s73, 1\n\t"
    "s_cmp_eq_u32 s73, 0\n\t"
    "v_mfma_f32_16x16x32_bf16 v[76:79], a[28:31], a[88:91], 0\n\t"
    "s_cselect_b32 s71, s76, s75\n\t"
    "s_cselect_b32 s73, 3, s73\n\t"
    "v_mfma_f32_16x16x32_bf16 v[76:79], a[12:15], a[72:75], v[76:79]\n\t"
    "v_add_u32 v8, s71, v8\n\t"
    "v_add_u32 v9, s71, v9\n\t"
    "v_mfma_f32_16x16x32_bf16 v[92:95], a[12:15], a[76:79], 0\n\t"
    "v_add_u32 v10, s71, v10\n\t"
    "v_add_u32 v11, s71, v11\n\t"
    "v_mfma_f32_16x16x32_bf16 v[92:95], a[28:31], a[92:95], v[92:95]\n\t"
    "s_sub_u32 s74, s74, 1\n\t"
    "s_cmp_eq_u32 s74, 0\n\t"
    "v_mfma_f32_16x16x32_bf16 v[88:91], a[24:27], a[92:95], 0\n\t"
    "s_cselect_b32 s72, s78, s77\n\t"
    "s_cselect_b32 s74, 3, s74\n\t"
    "v_mfma_f32_16x16x32_bf16 v[88:91], a[8:11], a[76:79], v[88:91]\n\t"
    "v_add_u32 v12, s72, v12\n\t"
    "v_add_u32 v13, s72, v13\n\t"
    "v_mfma_f32_16x16x32_bf16 v[84:87], a[4:7], a[76:79], 0\n\t"
    "v_add_u32 v14, s72, v14\n\t"
    "v_add_u32 v15, s72, v15\n\t"
    "v_mfma_f32_16x16x32_bf16 v[84:87], a[20:23], a[92:95], v[84:87]\n\t"
    "s_sub_u32 s68, s68, 1\n\t"
    "s_branch Lloop1%=\n\t"
    "Lsw%=:\n\t"
    "s_mov_b64 s[44:45], s[36:37]\n\t"
    "s_mov_b64 s[38:39], s[36:37]\n\t"
    "s_mov_b32 s80, 128\n\t"
    "s_mov_b32 s87, 0\n\t"
    "s_mov_b32 s66, 3427929153\n\t"
    "s_mov_b32 s67, 65\n\t"
    "s_and_b32 s55, %[b], 1\n\t"
    "s_mul_i32 s55, s55, 2097152\n\t"
    "s_add_u32 s55, s55, 7340032\n\t"
    "s_mov_b32 s56, 262144\n\t"
    "s_add_u32 s40, s58, s55\n\t"
    "s_addc_u32 s41, s59, 0\n\t"
    "s_mov_b32 s60, 0\n\t"
    "s_mov_b32 s61, 15360\n\t"
    "s_mov_b32 s62, 30720\n\t"
    "s_mov_b32 s63, 46080\n\t"
    "v_mov_b32 v19, v164\n\t"
    "v_xor_b32 v20, 64, v164\n\t"
    "v_cmp_ne_u32 vcc, s27, v28\n\t"
    "s_nop 4\n\t"
    "s_cbranch_vccz Lswb%=\n\t"
    "s_mov_b32 s84, 0x20000\n\t"
    "Lspp%=:\n\t"
    "global_load_dword v28, v29, s[28:29] offset:0 sc1\n\t"
    "s_waitcnt vmcnt(0)\n\t"
    "v_cmp_ne_u32 vcc, s27, v28\n\t"
    "s_sub_u32 s84, s84, 1\n\t"
    "s_cbranch_scc1 Lsdp%=\n\t"
    "s_nop 2\n\t"
    "s_cbranch_vccz Lsdp%=\n\t"
    "s_sleep 2\n\t"
    "s_branch Lspp%=\n\t"
    "Lsdp%=:\n\t"
    "s_branch Lswb%=\n\t"
    "Lfl%=:\n\t"
    "global_load_dword v28, v29, s[28:29] sc1\n\t"
    "s_branch Lflb%=\n\t"
    "Lf2%=:\n\t"
    "global_load_dword v26, v29, s[28:29] offset:128 sc1\n\t"
    "s_branch Lf2b%=\n\t"
    "Lc2%=:\n\t"
    "v_cmp_ne_u32 vcc, s27, v26\n\t"
    "s_nop 4\n\t"
    "s_cbranch_vccz Lc2b%=\n\t"
    "s_mov_b32 s84, 0x20000\n\t"
    "Lspl%=:\n\t"
    "global_load_dword v26, v29, s[28:29] offset:128 sc1\n\t"
    "s_waitcnt vmcnt(0)\n\t"
    "v_cmp_ne_u32 vcc, s27, v26\n\t"
    "s_sub_u32 s84, s84, 1\n\t"
    "s_cbranch_scc1 Lsdl%=\n\t"
    "s_nop 2\n\t"
    "s_cbranch_vccz Lsdl%=\n\t"
    "s_sleep 2\n\t"
    "s_branch Lspl%=\n\t"
    "Lsdl%=:\n\t"
    "s_branch Lc2b%=\n\t"
    "Lloop1%=:\n\t"
    "v_mfma_f32_16x16x32_bf16 v[80:83], a[16:19], a[92:95], v[80:83]\n\t"
    "s_waitcnt vmcnt(12) lgkmcnt(0)\n\t"
    "v_mfma_f32_16x16x32_bf16 v[80:83], a[0:3], a[76:79], v[80:83]\n\t"
    "s_barrier\n\t"
    "v_mfma_f32_16x16x32_bf16 v[96:99], a[0:3], a[96:99], v[96:99]\n\t"
    "ds_read_b64_tr_b16 a[32:33], v12 offset:0\n\t"
    "s_cmp_eq_u32 s68, 10\n\t"
    "v_mfma_f32_16x16x32_bf16 v[96:99], a[16:19], a[112:115], v[96:99]\n\t"
    "ds_read_b64_tr_b16 a[34:35], v12 offset:4096\n\t"
    "s_cbranch_scc1 Lsw%=\n\tLswb%=:\n\t"
    "v_mfma_f32_16x16x32_bf16 v[100:103], a[20:23], a[112:115], v[100:103]\n\t"
    "ds_read_b64_tr_b16 a[36:37], v13 offset:0\n\t"
    "s_mov_b32 m0, s70\n\t"
    "v_mfma_f32_16x16x32_bf16 v[100:103], a[4:7], a[96:99], v[100:103]\n\t"
    "buffer_load_dwordx4 v19, s[44:47], s60 offen offset:0 lds\n\t"
    "v_mfma_f32_16x16x32_bf16 v[104:107], a[8:11], a[96:99], v[104:107]\n\t"
    "ds_read_b64_tr_b16 a[38:39], v13 offset:4096\n\t"
    "ds_read_b64_tr_b16 a[40:41], v14 offset:0\n\t"
    "v_mfma_f32_16x16x32_bf16 v[104:107], a[24:27], a[112:115], v[104:107]\n\t"
    "ds_read_b64_tr_b16 a[42:43], v14 offset:4096\n\t"
    "ds_read_b64_tr_b16 a[44:45], v15 offset:0\n\t"
    "v_mfma_f32_16x16x32_bf16 v[108:111], a[28:31], a[112:115], v[108:111]\n\t"
    "ds_read_b64_tr_b16 a[46:47], v15 offset:4096\n\t"
    "ds_read_b64_tr_b16 a[48:49], v12 offset:8192\n\t"
    "v_mfma_f32_16x16x32_bf16 v[108:111], a[12:15], a[96:99], v[108:111]\n\t"
    "buffer_load_dwordx4 v20, s[44:47], s61 offen offset:1024 lds\n\t"
    "v_mfma_f32_16x16x32_bf16 v[124:127], a[12:15], a[100:103], v[124:127]\n\t"
    "ds_read_b64_tr_b16 a[50:51], v12 offset:12288\n\t"
    "ds_read_b64_tr_b16 a[52:53], v13 offset:8192\n\t"
    "v_mfma_f32_16x16x32_bf16 v[124:127], a[28:31], a[116:119], v[124:127]\n\t"
    "ds_read_b64_tr_b16 a[54:55], v13 offset:12288\n\t"
    "ds_read_b64_tr_b16 a[56:57], v14 offset:8192\n\t"
    "v_mfma_f32_16x16x32_bf16 v[120:123], a[24:27], a[116:119], v[120:123]\n\t"
    "ds_read_b64_tr_b16 a[58:59], v14 offset:12288\n\t"
    "ds_read_b64_tr_b16 a[60:61], v15 offset:8192\n\t"
    "v_mfma_f32_16x16x32_bf16 v[120:123], a[8:11], a[100:103], v[120:123]\n\t"
    "ds_read_b64_tr_b16 a[62:63], v15 offset:12288\n\t"
    "v_mfma_f32_16x16x32_bf16 v[116:119], a[4:7], a[100:103], v[116:119]\n\t"
    "buffer_load_dwordx4 v19, s[44:47], s62 offen offset:2048 lds\n\t"
    "v_mfma_f32_16x16x32_bf16 v[116:119], a[20:23], a[116:119], v[116:119]\n\t"
    "ds_read_b128 a[64:67], v8 offset:0\n\t"
    "v_mfma_f32_16x16x32_bf16 v[112:115], a[16:19], a[116:119], v[112:115]\n\t"
    "ds_read_b128 a[80:83], v8 offset:16384\n\t"
    "v_mfma_f32_16x16x32_bf16 v[112:115], a[0:3], a[100:103], v[112:115]\n\t"
    "ds_read_b128 a[68:71], v8 offset:1024\n\t"
    "v_mfma_f32_16x16x32_bf16 v[128:131], a[0:3], a[104:107], v[128:131]\n\t"
    "ds_read_b128 a[84:87], v8 offset:17408\n\t"
    "v_mfma_f32_16x16x32_bf16 v[128:131], a[16:19], a[120:123], v[128:131]\n\t"
    "buffer_load_dwordx4 v20, s[44:47], s63 offen offset:3072 lds\n\t"
    "v_mfma_f32_16x16x32_bf16 v[132:135], a[20:23], a[120:123], v[132:135]\n\t"
    "ds_read_b128 a[72:75], v9 offset:0\n\t"
    "s_and_b32 s44, s66, 31\n\t"
    "v_mfma_f32_16x16x32_bf16 v[132:135], a[4:7], a[104:107], v[132:135]\n\t"
    "ds_read_b128 a[88:91], v9 offset:16384\n\t"
    "s_lshr_b64 s[66:67], s[66:67], 5\n\t"
    "v_mfma_f32_16x16x32_bf16 v[136:139], a[8:11], a[104:107], v[136:139]\n\t"
    "ds_read_b128 a[76:79], v9 offset:1024\n\t"
    "s_mov_b32 m0, s69\n\t"
    "v_mfma_f32_16x16x32_bf16 v[136:139], a[24:27], a[120:123], v[136:139]\n\t"
    "buffer_load_dwordx4 v18, s[40:43], s52 offen offset:0 sc1 lds\n\t"
    "v_mfma_f32_16x16x32_bf16 v[140:143], a[28:31], a[120:123], v[140:143]\n\t"
    "ds_read_b128 a[92:95], v9 offset:17408\n\t"
    "s_mul_i32 s44, s44, s80\n\t"
    "v_mfma_f32_16x16x32_bf16 v[140:143], a[12:15], a[104:107], v[140:143]\n\t"
    "s_add_u32 s44, s44, s87\n\t"
    "s_add_u32 s44, s44, s38\n\t"
    "v_mfma_f32_16x16x32_bf16 v[156:159], a[12:15], a[108:111], v[156:159]\n\t"
    "s_addc_u32 s45, s39, 0\n\t"
    "s_add_u32 s70, s70, s72\n\t"
    "v_mfma_f32_16x16x32_bf16 v[156:159], a[28:31], a[124:127], v[156:159]\n\t"
    "s_sub_u32 s73, s73, 1\n\t"
    "s_cmp_eq_u32 s73, 0\n\t"
    "v_mfma_f32_16x16x32_bf16 v[152:155], a[24:27], a[124:127], v[152:155]\n\t"
    "buffer_load_dwordx4 v18, s[40:43], s52 offen offset:1024 sc1 lds\n\t"
    "v_mfma_f32_16x16x32_bf16 v[152:155], a[8:11], a[108:111], v[152:155]\n\t"
    "s_cselect_b32 s64, s76, s75\n\t"
    "s_cselect_b32 s73, 3, s73\n\t"
    "v_mfma_f32_16x16x32_bf16 v[148:151], a[4:7], a[108:111], v[148:151]\n\t"
    "s_sub_u32 s74, s74, 1\n\t"
    "s_cmp_eq_u32 s74, 0\n\t"
    "v_mfma_f32_16x16x32_bf16 v[148:151], a[20:23], a[124:127], v[148:151]\n\t"
    "s_cselect_b32 s65, s78, s77\n\t"
    "s_cselect_b32 s74, 3, s74\n\t"
    "v_mfma_f32_16x16x32_bf16 v[144:147], a[16:19], a[124:127], v[144:147]\n\t"
    "v_add_u32 v12, s65, v12\n\t"
    "v_add_u32 v13, s65, v13\n\t"
    "v_mfma_f32_16x16x32_bf16 v[144:147], a[0:3], a[108:111], v[144:147]\n\t"
    "buffer_load_dwordx4 v18, s[40:43], s52 offen offset:2048 sc1 lds\n\t"
    "s_waitcnt lgkmcnt(0)\n\t"
    "v_mfma_f32_16x16x32_bf16 v[32:35], a[32:35], a[64:67], v[32:35]\n\t"
    "ds_read_b128 a[96:99], v10 offset:0\n\t"
    "v_add_u32 v14, s65, v14\n\t"
    "v_mfma_f32_16x16x32_bf16 v[32:35], a[48:51], a[80:83], v[32:35]\n\t"
    "ds_read_b128 a[112:115], v10 offset:16384\n\t"
    "v_add_u32 v15, s65, v15\n\t"
    "v_mfma_f32_16x16x32_bf16 v[36:39], a[52:55], a[80:83], v[36:39]\n\t"
    "ds_read_b128 a[100:103], v10 offset:1024\n\t"
    "v_mfma_f32_16x16x32_bf16 v[36:39], a[36:39], a[64:67], v[36:39]\n\t"
    "buffer_load_dwordx4 v18, s[40:43], s52 offen offset:3072 sc1 lds\n\t"
    "v_mfma_f32_16x16x32_bf16 v[40:43], a[40:43], a[64:67], v[40:43]\n\t"
    "ds_read_b128 a[116:119], v10 offset:17408\n\t"
    "v_mfma_f32_16x16x32_bf16 v[40:43], a[56:59], a[80:83], v[40:43]\n\t"
    "ds_read_b128 a[104:107], v11 offset:0\n\t"
    "v_mfma_f32_16x16x32_bf16 v[44:47], a[60:63], a[80:83], v[44:47]\n\t"
    "ds_read_b128 a[120:123], v11 offset:16384\n\t"
    "v_mfma_f32_16x16x32_bf16 v[44:47], a[44:47], a[64:67], v[44:47]\n\t"
    "ds_read_b128 a[108:111], v11 offset:1024\n\t"
    "s_bitset1_b32 m0, 14\n\t"
    "v_mfma_f32_16x16x32_bf16 v[60:63], a[44:47], a[68:71], v[60:63]\n\t"
    "buffer_load_dwordx4 v18, s[40:43], s53 offen offset:0 sc1 lds\n\t"
    "v_mfma_f32_16x16x32_bf16 v[60:63], a[60:63], a[84:87], v[60:63]\n\t"
    "ds_read_b128 a[124:127], v11 offset:17408\n\t"
    "v_mfma_f32_16x16x32_bf16 v[56:59], a[56:59], a[84:87], v[56:59]\n\t"
    "v_add_u32 v8, s64, v8\n\t"
    "v_add_u32 v9, s64, v9\n\t"
    "v_mfma_f32_16x16x32_bf16 v[56:59], a[40:43], a[68:71], v[56:59]\n\t"
    "v_add_u32 v10, s64, v10\n\t"
    "v_add_u32 v11, s64, v11\n\t"
    "v_mfma_f32_16x16x32_bf16 v[52:55], a[36:39], a[68:71], v[52:55]\n\t"
    "v_mfma_f32_16x16x32_bf16 v[52:55], a[52:55], a[84:87], v[52:55]\n\t"
    "buffer_load_dwordx4 v18, s[40:43], s53 offen offset:1024 sc1 lds\n\t"
    "v_mfma_f32_16x16x32_bf16 v[48:51], a[48:51], a[84:87], v[48:51]\n\t"
    "v_mfma_f32_16x16x32_bf16 v[48:51], a[32:35], a[68:71], v[48:51]\n\t"
    "v_mfma_f32_16x16x32_bf16 v[64:67], a[32:35], a[72:75], v[64:67]\n\t"
    "v_mfma_f32_16x16x32_bf16 v[64:67], a[48:51], a[88:91], v[64:67]\n\t"
    "buffer_load_dwordx4 v18, s[40:43], s53 offen offset:2048 sc1 lds\n\t"
    "v_mfma_f32_16x16x32_bf16 v[68:71], a[52:55], a[88:91], v[68:71]\n\t"
    "v_mfma_f32_16x16x32_bf16 v[68:71], a[36:39], a[72:75], v[68:71]\n\t"
    "v_mfma_f32_16x16x32_bf16 v[72:75], a[40:43], a[72:75], v[72:75]\n\t"
    "v_mfma_f32_16x16x32_bf16 v[72:75], a[56:59], a[88:91], v[72:75]\n\t"
    "v_mfma_f32_16x16x32_bf16 v[76:79], a[60:63], a[88:91], v[76:79]\n\t"
    "buffer_load_dwordx4 v18, s[40:43], s53 offen offset:3072 sc1 lds\n\t"
    "v_mfma_f32_16x16x32_bf16 v[76:79], a[44:47], a[72:75], v[76:79]\n\t"
    "s_cmp_eq_u32 s68, s79\n\t"
    "s_cbranch_scc1 Lf2%=\n\tLf2b%=:\n\t"
    "v_mfma_f32_16x16x32_bf16 v[92:95], a[44:47], a[76:79], v[92:95]\n\t"
    "s_add_u32 s55, s55, s56\n\t"
    "s_add_u32 s40, s58, s55\n\t"
    "v_mfma_f32_16x16x32_bf16 v[92:95], a[60:63], a[92:95], v[92:95]\n\t"
    "s_addc_u32 s41, s59, 0\n\t"
    "s_add_u32 s69, s69, s71\n\t"
    "v_mfma_f32_16x16x32_bf16 v[88:91], a[56:59], a[92:95], v[88:91]\n\t"
    "s_sub_u32 s68, s68, 1\n\t"
    "v_mfma_f32_16x16x32_bf16 v[88:91], a[40:43], a[76:79], v[88:91]\n\t"
    "v_mfma_f32_16x16x32_bf16 v[84:87], a[36:39], a[76:79], v[84:87]\n\t"
    "v_mfma_f32_16x16x32_bf16 v[84:87], a[52:55], a[92:95], v[84:87]\n\t"
    "v_mfma_f32_16x16x32_bf16 v[80:83], a[48:51], a[92:95], v[80:83]\n\t"
    "s_waitcnt vmcnt(12) lgkmcnt(0)\n\t"
    "v_mfma_f32_16x16x32_bf16 v[80:83], a[32:35], a[76:79], v[80:83]\n\t"
    "s_barrier\n\t"
    "v_mfma_f32_16x16x32_bf16 v[96:99], a[32:35], a[96:99], v[96:99]\n\t"
    "ds_read_b64_tr_b16 a[0:1], v12 offset:0\n\t"
    "s_cmp_eq_u32 s68, s57\n\t"
    "v_mfma_f32_16x16x32_bf16 v[96:99], a[48:51], a[112:115], v[96:99]\n\t"
    "ds_read_b64_tr_b16 a[2:3], v12 offset:4096\n\t"
    "s_cbranch_scc1 Lc2%=\n\tLc2b%=:\n\t"
    "v_mfma_f32_16x16x32_bf16 v[100:103], a[52:55], a[112:115], v[100:103]\n\t"
    "ds_read_b64_tr_b16 a[4:5], v13 offset:0\n\t"
    "s_mov_b32 m0, s70\n\t"
    "v_mfma_f32_16x16x32_bf16 v[100:103], a[36:39], a[96:99], v[100:103]\n\t"
    "buffer_load_dwordx4 v19, s[44:47], s60 offen offset:0 lds\n\t"
    "v_mfma_f32_16x16x32_bf16 v[104:107], a[40:43], a[96:99], v[104:107]\n\t"
    "ds_read_b64_tr_b16 a[6:7], v13 offset:4096\n\t"
    "ds_read_b64_tr_b16 a[8:9], v14 offset:0\n\t"
    "v_mfma_f32_16x16x32_bf16 v[104:107], a[56:59], a[112:115], v[104:107]\n\t"
    "ds_read_b64_tr_b16 a[10:11], v14 offset:4096\n\t"
    "ds_read_b64_tr_b16 a[12:13], v15 offset:0\n\t"
    "v_mfma_f32_16x16x32_bf16 v[108:111], a[60:63], a[112:115], v[108:111]\n\t"
    "ds_read_b64_tr_b16 a[14:15], v15 offset:4096\n\t"
    "ds_read_b64_tr_b16 a[16:17], v12 offset:8192\n\t"
    "v_mfma_f32_16x16x32_bf16 v[108:111], a[44:47], a[96:99], v[108:111]\n\t"
    "buffer_load_dwordx4 v20, s[44:47], s61 offen offset:1024 lds\n\t"
    "v_mfma_f32_16x16x32_bf16 v[124:127], a[44:47], a[100:103], v[124:127]\n\t"
    "ds_read_b64_tr_b16 a[18:19], v12 offset:12288\n\t"
    "ds_read_b64_tr_b16 a[20:21], v13 offset:8192\n\t"
    "v_mfma_f32_16x16x32_bf16 v[124:127], a[60:63], a[116:119], v[124:127]\n\t"
    "ds_read_b64_tr_b16 a[22:23], v13 offset:12288\n\t"
    "ds_read_b64_tr_b16 a[24:25], v14 offset:8192\n\t"
    "v_mfma_f32_16x16x32_bf16 v[120:123], a[56:59], a[116:119], v[120:123]\n\t"
    "ds_read_b64_tr_b16 a[26:27], v14 offset:12288\n\t"
    "ds_read_b64_tr_b16 a[28:29], v15 offset:8192\n\t"
    "v_mfma_f32_16x16x32_bf16 v[120:123], a[40:43], a[100:103], v[120:123]\n\t"
    "ds_read_b64_tr_b16 a[30:31], v15 offset:12288\n\t"
    "v_mfma_f32_16x16x32_bf16 v[116:119], a[36:39], a[100:103], v[116:119]\n\t"
    "buffer_load_dwordx4 v19, s[44:47], s62 offen offset:2048 lds\n\t"
    "v_mfma_f32_16x16x32_bf16 v[116:119], a[52:55], a[116:119], v[116:119]\n\t"
    "ds_read_b128 a[64:67], v8 offset:0\n\t"
    "v_mfma_f32_16x16x32_bf16 v[112:115], a[48:51], a[116:119], v[112:115]\n\t"
    "ds_read_b128 a[80:83], v8 offset:16384\n\t"
    "v_mfma_f32_16x16x32_bf16 v[112:115], a[32:35], a[100:103], v[112:115]\n\t"
    "ds_read_b128 a[68:71], v8 offset:1024\n\t"
    "v_mfma_f32_16x16x32_bf16 v[128:131], a[32:35], a[104:107], v[128:131]\n\t"
    "ds_read_b128 a[84:87], v8 offset:17408\n\t"
    "v_mfma_f32_16x16x32_bf16 v[128:131], a[48:51], a[120:123], v[128:131]\n\t"
    "buffer_load_dwordx4 v20, s[44:47], s63 offen offset:3072 lds\n\t"
    "v_mfma_f32_16x16x32_bf16 v[132:135], a[52:55], a[120:123], v[132:135]\n\t"
    "ds_read_b128 a[72:75], v9 offset:0\n\t"
    "s_and_b32 s44, s66, 31\n\t"
    "v_mfma_f32_16x16x32_bf16 v[132:135], a[36:39], a[104:107], v[132:135]\n\t"
    "ds_read_b128 a[88:91], v9 offset:16384\n\t"
    "s_lshr_b64 s[66:67], s[66:67], 5\n\t"
    "v_mfma_f32_16x16x32_bf16 v[136:139], a[40:43], a[104:107], v[136:139]\n\t"
    "ds_read_b128 a[76:79], v9 offset:1024\n\t"
    "s_mov_b32 m0, s69\n\t"
    "v_mfma_f32_16x16x32_bf16 v[136:139], a[56:59], a[120:123], v[136:139]\n\t"
    "buffer_load_dwordx4 v18, s[40:43], s52 offen offset:0 sc1 lds\n\t"
    "v_mfma_f32_16x16x32_bf16 v[140:143], a[60:63], a[120:123], v[140:143]\n\t"
    "ds_read_b128 a[92:95], v9 offset:17408\n\t"
    "s_mul_i32 s44, s44, s80\n\t"
    "v_mfma_f32_16x16x32_bf16 v[140:143], a[44:47], a[104:107], v[140:143]\n\t"
    "s_add_u32 s44, s44, s87\n\t"
    "s_add_u32 s44, s44, s38\n\t"
    "v_mfma_f32_16x16x32_bf16 v[156:159], a[44:47], a[108:111], v[156:159]\n\t"
    "s_addc_u32 s45, s39, 0\n\t"
    "s_add_u32 s70, s70, s65\n\t"
    "v_mfma_f32_16x16x32_bf16 v[156:159], a[60:63], a[124:127], v[156:159]\n\t"
    "s_sub_u32 s73, s73, 1\n\t"
    "s_cmp_eq_u32 s73, 0\n\t"
    "v_mfma_f32_16x16x32_bf16 v[152:155], a[56:59], a[124:127], v[152:155]\n\t"
    "buffer_load_dwordx4 v18, s[40:43], s52 offen offset:1024 sc1 lds\n\t"
    "v_mfma_f32_16x16x32_bf16 v[152:155], a[40:43], a[108:111], v[152:155]\n\t"
    "s_cselect_b32 s71, s76, s75\n\t"
    "s_cselect_b32 s73, 3, s73\n\t"
    "v_mfma_f32_16x16x32_bf16 v[148:151], a[36:39], a[108:111], v[148:151]\n\t"
    "s_sub_u32 s74, s74, 1\n\t"
    "s_cmp_eq_u32 s74, 0\n\t"
    "v_mfma_f32_16x16x32_bf16 v[148:151], a[52:55], a[124:127], v[148:151]\n\t"
    "s_cselect_b32 s72, s78, s77\n\t"
    "s_cselect_b32 s74, 3, s74\n\t"
    "v_mfma_f32_16x16x32_bf16 v[144:147], a[48:51], a[124:127], v[144:147]\n\t"
    "v_add_u32 v12, s72, v12\n\t"
    "v_add_u32 v13, s72, v13\n\t"
    "v_mfma_f32_16x16x32_bf16 v[144:147], a[32:35], a[108:111], v[144:147]\n\t"
    "buffer_load_dwordx4 v18, s[40:43], s52 offen offset:2048 sc1 lds\n\t"
    "s_waitcnt lgkmcnt(0)\n\t"
    "v_mfma_f32_16x16x32_bf16 v[32:35], a[0:3], a[64:67], v[32:35]\n\t"
    "ds_read_b128 a[96:99], v10 offset:0\n\t"
    "v_add_u32 v14, s72, v14\n\t"
    "v_mfma_f32_16x16x32_bf16 v[32:35], a[16:19], a[80:83], v[32:35]\n\t"
    "ds_read_b128 a[112:115], v10 offset:16384\n\t"
    "v_add_u32 v15, s72, v15\n\t"
    "v_mfma_f32_16x16x32_bf16 v[36:39], a[20:23], a[80:83], v[36:39]\n\t"
    "ds_read_b128 a[100:103], v10 offset:1024\n\t"
    "v_mfma_f32_16x16x32_bf16 v[36:39], a[4:7], a[64:67], v[36:39]\n\t"
    "buffer_load_dwordx4 v18, s[40:43], s52 offen offset:3072 sc1 lds\n\t"
    "v_mfma_f32_16x16x32_bf16 v[40:43], a[8:11], a[64:67], v[40:43]\n\t"
    "ds_read_b128 a[116:119], v10 offset:17408\n\t"
    "v_mfma_f32_16x16x32_bf16 v[40:43], a[24:27], a[80:83], v[40:43]\n\t"
    "ds_read_b128 a[104:107], v11 offset:0\n\t"
    "v_mfma_f32_16x16x32_bf16 v[44:47], a[28:31], a[80:83], v[44:47]\n\t"
    "ds_read_b128 a[120:123], v11 offset:16384\n\t"
    "v_mfma_f32_16x16x32_bf16 v[44:47], a[12:15], a[64:67], v[44:47]\n\t"
    "ds_read_b128 a[108:111], v11 offset:1024\n\t"
    "s_bitset1_b32 m0, 14\n\t"
    "v_mfma_f32_16x16x32_bf16 v[60:63], a[12:15], a[68:71], v[60:63]\n\t"
    "buffer_load_dwordx4 v18, s[40:43], s53 offen offset:0 sc1 lds\n\t"
    "v_mfma_f32_16x16x32_bf16 v[60:63], a[28:31], a[84:87], v[60:63]\n\t"
    "ds_read_b128 a[124:127], v11 offset:17408\n\t"
    "v_mfma_f32_16x16x32_bf16 v[56:59], a[24:27], a[84:87], v[56:59]\n\t"
    "v_add_u32 v8, s71, v8\n\t"
    "v_add_u32 v9, s71, v9\n\t"
    "v_mfma_f32_16x16x32_bf16 v[56:59], a[8:11], a[68:71], v[56:59]\n\t"
    "v_add_u32 v10, s71, v10\n\t"
    "v_add_u32 v11, s71, v11\n\t"
    "v_mfma_f32_16x16x32_bf16 v[52:55], a[4:7], a[68:71], v[52:55]\n\t"
    "v_mfma_f32_16x16x32_bf16 v[52:55], a[20:23], a[84:87], v[52:55]\n\t"
    "buffer_load_dwordx4 v18, s[40:43], s53 offen offset:1024 sc1 lds\n\t"
    "v_mfma_f32_16x16x32_bf16 v[48:51], a[16:19], a[84:87], v[48:51]\n\t"
    "v_mfma_f32_16x16x32_bf16 v[48:51], a[0:3], a[68:71], v[48:51]\n\t"
    "v_mfma_f32_16x16x32_bf16 v[64:67], a[0:3], a[72:75], v[64:67]\n\t"
    "v_mfma_f32_16x16x32_bf16 v[64:67], a[16:19], a[88:91], v[64:67]\n\t"
    "buffer_load_dwordx4 v18, s[40:43], s53 offen offset:2048 sc1 lds\n\t"
    "v_mfma_f32_16x16x32_bf16 v[68:71], a[20:23], a[88:91], v[68:71]\n\t"
    "v_mfma_f32_16x16x32_bf16 v[68:71], a[4:7], a[72:75], v[68:71]\n\t"
    "v_mfma_f32_16x16x32_bf16 v[72:75], a[8:11], a[72:75], v[72:75]\n\t"
    "v_mfma_f32_16x16x32_bf16 v[72:75], a[24:27], a[88:91], v[72:75]\n\t"
    "v_mfma_f32_16x16x32_bf16 v[76:79], a[28:31], a[88:91], v[76:79]\n\t"
    "buffer_load_dwordx4 v18, s[40:43], s53 offen offset:3072 sc1 lds\n\t"
    "v_mfma_f32_16x16x32_bf16 v[76:79], a[12:15], a[72:75], v[76:79]\n\t"
    "s_cmp_eq_u32 s68, 13\n\t"
    "s_cbranch_scc1 Lfl%=\n\tLflb%=:\n\t"
    "v_mfma_f32_16x16x32_bf16 v[92:95], a[12:15], a[76:79], v[92:95]\n\t"
    "s_add_u32 s55, s55, s56\n\t"
    "s_add_u32 s40, s58, s55\n\t"
    "v_mfma_f32_16x16x32_bf16 v[92:95], a[28:31], a[92:95], v[92:95]\n\t"
    "s_addc_u32 s41, s59, 0\n\t"
    "s_add_u32 s69, s69, s64\n\t"
    "v_mfma_f32_16x16x32_bf16 v[88:91], a[24:27], a[92:95], v[88:91]\n\t"
    "s_sub_u32 s68, s68, 1\n\t"
    "s_cmp_lg_u32 s68, 8\n\t"
    "v_mfma_f32_16x16x32_bf16 v[88:91], a[8:11], a[76:79], v[88:91]\n\t"
    "v_mfma_f32_16x16x32_bf16 v[84:87], a[4:7], a[76:79], v[84:87]\n\t"
    "v_mfma_f32_16x16x32_bf16 v[84:87], a[20:23], a[92:95], v[84:87]\n\t"
    "s_cbranch_scc1 Lloop1%=\n\t"
    "Lout1%=:\n\t"
    "v_add_u32 v12, v12, v160\n\t"
    "v_xor_b32 v13, 32, v12\n\t"
    "v_xor_b32 v14, 64, v12\n\t"
    "v_xor_b32 v15, 96, v12\n\t"
    "Lloop2%=:\n\t"
    "v_mfma_f32_16x16x32_bf16 v[80:83], a[16:19], a[92:95], v[80:83]\n\t"
    "s_waitcnt vmcnt(12) lgkmcnt(0)\n\t"
    "v_mfma_f32_16x16x32_bf16 v[80:83], a[0:3], a[76:79], v[80:83]\n\t"
    "s_barrier\n\t"
    "v_mfma_f32_16x16x32_bf16 v[96:99], a[0:3], a[96:99], v[96:99]\n\t"
    "ds_read_b64 a[32:33], v12 offset:0\n\t"
    "v_mfma_f32_16x16x32_bf16 v[96:99], a[16:19], a[112:115], v[96:99]\n\t"
    "ds_read_b64 a[34:35], v13 offset:0\n\t"
    "v_mfma_f32_16x16x32_bf16 v[100:103], a[20:23], a[112:115], v[100:103]\n\t"
    "ds_read_b64 a[36:37], v12 offset:2048\n\t"
    "s_mov_b32 m0, s70\n\t"
    "v_mfma_f32_16x16x32_bf16 v[100:103], a[4:7], a[96:99], v[100:103]\n\t"
    "buffer_load_dwordx4 v19, s[44:47], s60 offen offset:0 lds\n\t"
    "v_mfma_f32_16x16x32_bf16 v[104:107], a[8:11], a[96:99], v[104:107]\n\t"
    "ds_read_b64 a[38:39], v13 offset:2048\n\t"
    "ds_read_b64 a[40:41], v12 offset:4096\n\t"
    "v_mfma_f32_16x16x32_bf16 v[104:107], a[24:27], a[112:115], v[104:107]\n\t"
    "ds_read_b64 a[42:43], v13 offset:4096\n\t"
    "ds_read_b64 a[44:45], v12 offset:6144\n\t"
    "v_mfma_f32_16x16x32_bf16 v[108:111], a[28:31], a[112:115], v[108:111]\n\t"
    "ds_read_b64 a[46:47], v13 offset:6144\n\t"
    "ds_read_b64 a[48:49], v14 offset:0\n\t"
    "v_mfma_f32_16x16x32_bf16 v[108:111], a[12:15], a[96:99], v[108:111]\n\t"
    "buffer_load_dwordx4 v20, s[44:47], s61 offen offset:1024 lds\n\t"
    "v_mfma_f32_16x16x32_bf16 v[124:127], a[12:15], a[100:103], v[124:127]\n\t"
    "ds_read_b64 a[50:51], v15 offset:0\n\t"
    "ds_read_b64 a[52:53], v14 offset:2048\n\t"
    "v_mfma_f32_16x16x32_bf16 v[124:127], a[28:31], a[116:119], v[124:127]\n\t"
    "ds_read_b64 a[54:55], v15 offset:2048\n\t"
    "ds_read_b64 a[56:57], v14 offset:4096\n\t"
    "v_mfma_f32_16x16x32_bf16 v[120:123], a[24:27], a[116:119], v[120:123]\n\t"
    "ds_read_b64 a[58:59], v15 offset:4096\n\t"
    "ds_read_b64 a[60:61], v14 offset:6144\n\t"
    "v_mfma_f32_16x16x32_bf16 v[120:123], a[8:11], a[100:103], v[120:123]\n\t"
    "ds_read_b64 a[62:63], v15 offset:6144\n\t"
    "v_mfma_f32_16x16x32_bf16 v[116:119], a[4:7], a[100:103], v[116:119]\n\t"
    "buffer_load_dwordx4 v19, s[44:47], s62 offen offset:2048 lds\n\t"
    "v_mfma_f32_16x16x32_bf16 v[116:119], a[20:23], a[116:119], v[116:119]\n\t"
    "ds_read_b128 a[64:67], v8 offset:0\n\t"
    "v_mfma_f32_16x16x32_bf16 v[112:115], a[16:19], a[116:119], v[112:115]\n\t"
    "ds_read_b128 a[80:83], v8 offset:16384\n\t"
    "v_mfma_f32_16x16x32_bf16 v[112:115], a[0:3], a[100:103], v[112:115]\n\t"
    "ds_read_b128 a[68:71], v8 offset:1024\n\t"
    "v_mfma_f32_16x16x32_bf16 v[128:131], a[0:3], a[104:107], v[128:131]\n\t"
    "ds_read_b128 a[84:87], v8 offset:17408\n\t"
    "v_mfma_f32_16x16x32_bf16 v[128:131], a[16:19], a[120:123], v[128:131]\n\t"
    "buffer_load_dwordx4 v20, s[44:47], s63 offen offset:3072 lds\n\t"
    "v_mfma_f32_16x16x32_bf16 v[132:135], a[20:23], a[120:123], v[132:135]\n\t"
    "ds_read_b128 a[72:75], v9 offset:0\n\t"
    "s_and_b32 s44, s66, 31\n\t"
    "v_mfma_f32_16x16x32_bf16 v[132:135], a[4:7], a[104:107], v[132:135]\n\t"
    "ds_read_b128 a[88:91], v9 offset:16384\n\t"
    "s_lshr_b64 s[66:67], s[66:67], 5\n\t"
    "v_mfma_f32_16x16x32_bf16 v[136:139], a[8:11], a[104:107], v[136:139]\n\t"
    "ds_read_b128 a[76:79], v9 offset:1024\n\t"
    "s_mov_b32 m0, s69\n\t"
    "v_mfma_f32_16x16x32_bf16 v[136:139], a[24:27], a[120:123], v[136:139]\n\t"
    "buffer_load_dwordx4 v18, s[40:43], s52 offen offset:0 sc1 lds\n\t"
    "v_mfma_f32_16x16x32_bf16 v[140:143], a[28:31], a[120:123], v[140:143]\n\t"
    "ds_read_b128 a[92:95], v9 offset:17408\n\t"
    "s_mul_i32 s44, s44, s80\n\t"
    "v_mfma_f32_16x16x32_bf16 v[140:143], a[12:15], a[104:107], v[140:143]\n\t"
    "s_add_u32 s44, s44, s87\n\t"
    "s_add_u32 s44, s44, s38\n\t"
    "v_mfma_f32_16x16x32_bf16 v[156:159], a[12:15], a[108:111], v[156:159]\n\t"
    "s_addc_u32 s45, s39, 0\n\t"
    "s_add_u32 s70, s70, s72\n\t"
    "v_mfma_f32_16x16x32_bf16 v[156:159], a[28:31], a[124:127], v[156:159]\n\t"
    "s_sub_u32 s73, s73, 1\n\t"
    "s_cmp_eq_u32 s73, 0\n\t"
    "v_mfma_f32_16x16x32_bf16 v[152:155], a[24:27], a[124:127], v[152:155]\n\t"
    "buffer_load_dwordx4 v18, s[40:43], s52 offen offset:1024 sc1 lds\n\t"
    "v_mfma_f32_16x16x32_bf16 v[152:155], a[8:11], a[108:111], v[152:155]\n\t"
    "s_cselect_b32 s64, s76, s75\n\t"
    "s_cselect_b32 s73, 3, s73\n\t"
    "v_mfma_f32_16x16x32_bf16 v[148:151], a[4:7], a[108:111], v[148:151]\n\t"
    "s_sub_u32 s74, s74, 1\n\t"
    "s_cmp_eq_u32 s74, 0\n\t"
    "v_mfma_f32_16x16x32_bf16 v[148:151], a[20:23], a[124:127], v[148:151]\n\t"
    "s_cselect_b32 s65, s78, s77\n\t"
    "s_cselect_b32 s74, 3, s74\n\t"
    "v_mfma_f32_16x16x32_bf16 v[144:147], a[16:19], a[124:127], v[144:147]\n\t"
    "v_add_u32 v12, s65, v12\n\t"
    "v_add_u32 v13, s65, v13\n\t"
    "v_mfma_f32_16x16x32_bf16 v[144:147], a[0:3], a[108:111], v[144:147]\n\t"
    "buffer_load_dwordx4 v18, s[40:43], s52 offen offset:2048 sc1 lds\n\t"
    "s_waitcnt lgkmcnt(0)\n\t"
    "v_mfma_f32_16x16x32_bf16 v[32:35], a[32:35], a[64:67], v[32:35]\n\t"
    "ds_read_b128 a[96:99], v10 offset:0\n\t"
    "v_add_u32 v14, s65, v14\n\t"
    "v_mfma_f32_16x16x32_bf16 v[32:35], a[48:51], a[80:83], v[32:35]\n\t"
    "ds_read_b128 a[112:115], v10 offset:16384\n\t"
    "v_add_u32 v15, s65, v15\n\t"
    "v_mfma_f32_16x16x32_bf16 v[36:39], a[52:55], a[80:83], v[36:39]\n\t"
    "ds_read_b128 a[100:103], v10 offset:1024\n\t"
    "v_mfma_f32_16x16x32_bf16 v[36:39], a[36:39], a[64:67], v[36:39]\n\t"
    "buffer_load_dwordx4 v18, s[40:43], s52 offen offset:3072 sc1 lds\n\t"
    "v_mfma_f32_16x16x32_bf16 v[40:43], a[40:43], a[64:67], v[40:43]\n\t"
    "ds_read_b128 a[116:119], v10 offset:17408\n\t"
    "v_mfma_f32_16x16x32_bf16 v[40:43], a[56:59], a[80:83], v[40:43]\n\t"
    "ds_read_b128 a[104:107], v11 offset:0\n\t"
    "v_mfma_f32_16x16x32_bf16 v[44:47], a[60:63], a[80:83], v[44:47]\n\t"
    "ds_read_b128 a[120:123], v11 offset:16384\n\t"
    "v_mfma_f32_16x16x32_bf16 v[44:47], a[44:47], a[64:67], v[44:47]\n\t"
    "ds_read_b128 a[108:111], v11 offset:1024\n\t"
    "s_bitset1_b32 m0, 14\n\t"
    "v_mfma_f32_16x16x32_bf16 v[60:63], a[44:47], a[68:71], v[60:63]\n\t"
    "buffer_load_dwordx4 v18, s[40:43], s53 offen offset:0 sc1 lds\n\t"
    "v_mfma_f32_16x16x32_bf16 v[60:63], a[60:63], a[84:87], v[60:63]\n\t"
    "ds_read_b128 a[124:127], v11 offset:17408\n\t"
    "v_mfma_f32_16x16x32_bf16 v[56:59], a[56:59], a[84:87], v[56:59]\n\t"
    "v_add_u32 v8, s64, v8\n\t"
    "v_add_u32 v9, s64, v9\n\t"
    "v_mfma_f32_16x16x32_bf16 v[56:59], a[40:43], a[68:71], v[56:59]\n\t"
    "v_add_u32 v10, s64, v10\n\t"
    "v_add_u32 v11, s64, v11\n\t"
    "v_mfma_f32_16x16x32_bf16 v[52:55], a[36:39], a[68:71], v[52:55]\n\t"
    "v_mfma_f32_16x16x32_bf16 v[52:55], a[52:55], a[84:87], v[52:55]\n\t"
    "buffer_load_dwordx4 v18, s[40:43], s53 offen offset:1024 sc1 lds\n\t"
    "v_mfma_f32_16x16x32_bf16 v[48:51], a[48:51], a[84:87], v[48:51]\n\t"
    "v_mfma_f32_16x16x32_bf16 v[48:51], a[32:35], a[68:71], v[48:51]\n\t"
    "v_mfma_f32_16x16x32_bf16 v[64:67], a[32:35], a[72:75], v[64:67]\n\t"
    "v_mfma_f32_16x16x32_bf16 v[64:67], a[48:51], a[88:91], v[64:67]\n\t"
    "buffer_load_dwordx4 v18, s[40:43], s53 offen offset:2048 sc1 lds\n\t"
    "v_mfma_f32_16x16x32_bf16 v[68:71], a[52:55], a[88:91], v[68:71]\n\t"
    "v_mfma_f32_16x16x32_bf16 v[68:71], a[36:39], a[72:75], v[68:71]\n\t"
    "v_mfma_f32_16x16x32_bf16 v[72:75], a[40:43], a[72:75], v[72:75]\n\t"
    "v_mfma_f32_16x16x32_bf16 v[72:75], a[56:59], a[88:91], v[72:75]\n\t"
    "v_mfma_f32_16x16x32_bf16 v[76:79], a[60:63], a[88:91], v[76:79]\n\t"
    "buffer_load_dwordx4 v18, s[40:43], s53 offen offset:3072 sc1 lds\n\t"
    "v_mfma_f32_16x16x32_bf16 v[76:79], a[44:47], a[72:75], v[76:79]\n\t"
    "s_add_u32 s55, s55, s56\n\t"
    "s_add_u32 s40, s58, s55\n\t"
    "v_mfma_f32_16x16x32_bf16 v[92:95], a[44:47], a[76:79], v[92:95]\n\t"
    "s_addc_u32 s41, s59, 0\n\t"
    "s_add_u32 s69, s69, s71\n\t"
    "v_mfma_f32_16x16x32_bf16 v[92:95], a[60:63], a[92:95], v[92:95]\n\t"
    "s_sub_u32 s68, s68, 1\n\t"
    "v_mfma_f32_16x16x32_bf16 v[88:91], a[56:59], a[92:95], v[88:91]\n\t"
    "v_mfma_f32_16x16x32_bf16 v[88:91], a[40:43], a[76:79], v[88:91]\n\t"
    "v_mfma_f32_16x16x32_bf16 v[84:87], a[36:39], a[76:79], v[84:87]\n\t"
    "v_mfma_f32_16x16x32_bf16 v[84:87], a[52:55], a[92:95], v[84:87]\n\t"
    "v_mfma_f32_16x16x32_bf16 v[80:83], a[48:51], a[92:95], v[80:83]\n\t"
    "s_waitcnt vmcnt(12) lgkmcnt(0)\n\t"
    "v_mfma_f32_16x16x32_bf16 v[80:83], a[32:35], a[76:79], v[80:83]\n\t"
    "s_barrier\n\t"
    "v_mfma_f32_16x16x32_bf16 v[96:99], a[32:35], a[96:99], v[96:99]\n\t"
    "ds_read_b64 a[0:1], v12 offset:0\n\t"
    "v_mfma_f32_16x16x32_bf16 v[96:99], a[48:51], a[112:115], v[96:99]\n\t"
    "ds_read_b64 a[2:3], v13 offset:0\n\t"
    "v_mfma_f32_16x16x32_bf16 v[100:103], a[52:55], a[112:115], v[100:103]\n\t"
    "ds_read_b64 a[4:5], v12 offset:2048\n\t"
    "s_mov_b32 m0, s70\n\t"
    "v_mfma_f32_16x16x32_bf16 v[100:103], a[36:39], a[96:99], v[100:103]\n\t"
    "buffer_load_dwordx4 v19, s[44:47], s60 offen offset:0 lds\n\t"
    "v_mfma_f32_16x16x32_bf16 v[104:107], a[40:43], a[96:99], v[104:107]\n\t"
    "ds_read_b64 a[6:7], v13 offset:2048\n\t"
    "ds_read_b64 a[8:9], v12 offset:4096\n\t"
    "v_mfma_f32_16x16x32_bf16 v[104:107], a[56:59], a[112:115], v[104:107]\n\t"
    "ds_read_b64 a[10:11], v13 offset:4096\n\t"
    "ds_read_b64 a[12:13], v12 offset:6144\n\t"
    "v_mfma_f32_16x16x32_bf16 v[108:111], a[60:63], a[112:115], v[108:111]\n\t"
    "ds_read_b64 a[14:15], v13 offset:6144\n\t"
    "ds_read_b64 a[16:17], v14 offset:0\n\t"
    "v_mfma_f32_16x16x32_bf16 v[108:111], a[44:47], a[96:99], v[108:111]\n\t"
    "buffer_load_dwordx4 v20, s[44:47], s61 offen offset:1024 lds\n\t"
    "v_mfma_f32_16x16x32_bf16 v[124:127], a[44:47], a[100:103], v[124:127]\n\t"
    "ds_read_b64 a[18:19], v15 offset:0\n\t"
    "ds_read_b64 a[20:21], v14 offset:2048\n\t"
    "v_mfma_f32_16x16x32_bf16 v[124:127], a[60:63], a[116:119], v[124:127]\n\t"
    "ds_read_b64 a[22:23], v15 offset:2048\n\t"
    "ds_read_b64 a[24:25], v14 offset:4096\n\t"
    "v_mfma_f32_16x16x32_bf16 v[120:123], a[56:59], a[116:119], v[120:123]\n\t"
    "ds_read_b64 a[26:27], v15 offset:4096\n\t"
    "ds_read_b64 a[28:29], v14 offset:6144\n\t"
    "v_mfma_f32_16x16x32_bf16 v[120:123], a[40:43], a[100:103], v[120:123]\n\t"
    "ds_read_b64 a[30:31], v15 offset:6144\n\t"
    "v_mfma_f32_16x16x32_bf16 v[116:119], a[36:39], a[100:103], v[116:119]\n\t"
    "buffer_load_dwordx4 v19, s[44:47], s62 offen offset:2048 lds\n\t"
    "v_mfma_f32_16x16x32_bf16 v[116:119], a[52:55], a[116:119], v[116:119]\n\t"
    "ds_read_b128 a[64:67], v8 offset:0\n\t"
    "v_mfma_f32_16x16x32_bf16 v[112:115], a[48:51], a[116:119], v[112:115]\n\t"
    "ds_read_b128 a[80:83], v8 offset:16384\n\t"
    "v_mfma_f32_16x16x32_bf16 v[112:115], a[32:35], a[100:103], v[112:115]\n\t"
    "ds_read_b128 a[68:71], v8 offset:1024\n\t"
    "v_mfma_f32_16x16x32_bf16 v[128:131], a[32:35], a[104:107], v[128:131]\n\t"
    "ds_read_b128 a[84:87], v8 offset:17408\n\t"
    "v_mfma_f32_16x16x32_bf16 v[128:131], a[48:51], a[120:123], v[128:131]\n\t"
    "buffer_load_dwordx4 v20, s[44:47], s63 offen offset:3072 lds\n\t"
    "v_mfma_f32_16x16x32_bf16 v[132:135], a[52:55], a[120:123], v[132:135]\n\t"
    "ds_read_b128 a[72:75], v9 offset:0\n\t"
    "s_and_b32 s44, s66, 31\n\t"
    "v_mfma_f32_16x16x32_bf16 v[132:135], a[36:39], a[104:107], v[132:135]\n\t"
    "ds_read_b128 a[88:91], v9 offset:16384\n\t"
    "s_lshr_b64 s[66:67], s[66:67], 5\n\t"
    "v_mfma_f32_16x16x32_bf16 v[136:139], a[40:43], a[104:107], v[136:139]\n\t"
    "ds_read_b128 a[76:79], v9 offset:1024\n\t"
    "s_mov_b32 m0, s69\n\t"
    "v_mfma_f32_16x16x32_bf16 v[136:139], a[56:59], a[120:123], v[136:139]\n\t"
    "buffer_load_dwordx4 v18, s[40:43], s52 offen offset:0 sc1 lds\n\t"
    "v_mfma_f32_16x16x32_bf16 v[140:143], a[60:63], a[120:123], v[140:143]\n\t"
    "ds_read_b128 a[92:95], v9 offset:17408\n\t"
    "s_mul_i32 s44, s44, s80\n\t"
    "v_mfma_f32_16x16x32_bf16 v[140:143], a[44:47], a[104:107], v[140:143]\n\t"
    "s_add_u32 s44, s44, s87\n\t"
    "s_add_u32 s44, s44, s38\n\t"
    "v_mfma_f32_16x16x32_bf16 v[156:159], a[44:47], a[108:111], v[156:159]\n\t"
    "s_addc_u32 s45, s39, 0\n\t"
    "s_add_u32 s70, s70, s65\n\t"
    "v_mfma_f32_16x16x32_bf16 v[156:159], a[60:63], a[124:127], v[156:159]\n\t"
    "s_sub_u32 s73, s73, 1\n\t"
    "s_cmp_eq_u32 s73, 0\n\t"
    "v_mfma_f32_16x16x32_bf16 v[152:155], a[56:59], a[124:127], v[152:155]\n\t"
    "buffer_load_dwordx4 v18, s[40:43], s52 offen offset:1024 sc1 lds\n\t"
    "v_mfma_f32_16x16x32_bf16 v[152:155], a[40:43], a[108:111], v[152:155]\n\t"
    "s_cselect_b32 s71, s76, s75\n\t"
    "s_cselect_b32 s73, 3, s73\n\t"
    "v_mfma_f32_16x16x32_bf16 v[148:151], a[36:39], a[108:111], v[148:151]\n\t"
    "s_sub_u32 s74, s74, 1\n\t"
    "s_cmp_eq_u32 s74, 0\n\t"
    "v_mfma_f32_16x16x32_bf16 v[148:151], a[52:55], a[124:127], v[148:151]\n\t"
    "s_cselect_b32 s72, s78, s77\n\t"
    "s_cselect_b32 s74, 3, s74\n\t"
    "v_mfma_f32_16x16x32_bf16 v[144:147], a[48:51], a[124:127], v[144:147]\n\t"
    "v_add_u32 v12, s72, v12\n\t"
    "v_add_u32 v13, s72, v13\n\t"
    "v_mfma_f32_16x16x32_bf16 v[144:147], a[32:35], a[108:111], v[144:147]\n\t"
    "buffer_load_dwordx4 v18, s[40:43], s52 offen offset:2048 sc1 lds\n\t"
    "s_waitcnt lgkmcnt(0)\n\t"
    "v_mfma_f32_16x16x32_bf16 v[32:35], a[0:3], a[64:67], v[32:35]\n\t"
    "ds_read_b128 a[96:99], v10 offset:0\n\t"
    "v_add_u32 v14, s72, v14\n\t"
    "v_mfma_f32_16x16x32_bf16 v[32:35], a[16:19], a[80:83], v[32:35]\n\t"
    "ds_read_b128 a[112:115], v10 offset:16384\n\t"
    "v_add_u32 v15, s72, v15\n\t"
    "v_mfma_f32_16x16x32_bf16 v[36:39], a[20:23], a[80:83], v[36:39]\n\t"
    "ds_read_b128 a[100:103], v10 offset:1024\n\t"
    "v_mfma_f32_16x16x32_bf16 v[36:39], a[4:7], a[64:67], v[36:39]\n\t"
    "buffer_load_dwordx4 v18, s[40:43], s52 offen offset:3072 sc1 lds\n\t"
    "v_mfma_f32_16x16x32_bf16 v[40:43], a[8:11], a[64:67], v[40:43]\n\t"
    "ds_read_b128 a[116:119], v10 offset:17408\n\t"
    "v_mfma_f32_16x16x32_bf16 v[40:43], a[24:27], a[80:83], v[40:43]\n\t"
    "ds_read_b128 a[104:107], v11 offset:0\n\t"
    "v_mfma_f32_16x16x32_bf16 v[44:47], a[28:31], a[80:83], v[44:47]\n\t"
    "ds_read_b128 a[120:123], v11 offset:16384\n\t"
    "v_mfma_f32_16x16x32_bf16 v[44:47], a[12:15], a[64:67], v[44:47]\n\t"
    "ds_read_b128 a[108:111], v11 offset:1024\n\t"
    "s_bitset1_b32 m0, 14\n\t"
    "v_mfma_f32_16x16x32_bf16 v[60:63], a[12:15], a[68:71], v[60:63]\n\t"
    "buffer_load_dwordx4 v18, s[40:43], s53 offen offset:0 sc1 lds\n\t"
    "v_mfma_f32_16x16x32_bf16 v[60:63], a[28:31], a[84:87], v[60:63]\n\t"
    "ds_read_b128 a[124:127], v11 offset:17408\n\t"
    "v_mfma_f32_16x16x32_bf16 v[56:59], a[24:27], a[84:87], v[56:59]\n\t"
    "v_add_u32 v8, s71, v8\n\t"
    "v_add_u32 v9, s71, v9\n\t"
    "v_mfma_f32_16x16x32_bf16 v[56:59], a[8:11], a[68:71], v[56:59]\n\t"
    "v_add_u32 v10, s71, v10\n\t"
    "v_add_u32 v11, s71, v11\n\t"
    "v_mfma_f32_16x16x32_bf16 v[52:55], a[4:7], a[68:71], v[52:55]\n\t"
    "v_mfma_f32_16x16x32_bf16 v[52:55], a[20:23], a[84:87], v[52:55]\n\t"
    "buffer_load_dwordx4 v18, s[40:43], s53 offen offset:1024 sc1 lds\n\t"
    "v_mfma_f32_16x16x32_bf16 v[48:51], a[16:19], a[84:87], v[48:51]\n\t"
    "v_mfma_f32_16x16x32_bf16 v[48:51], a[0:3], a[68:71], v[48:51]\n\t"
    "v_mfma_f32_16x16x32_bf16 v[64:67], a[0:3], a[72:75], v[64:67]\n\t"
    "v_mfma_f32_16x16x32_bf16 v[64:67], a[16:19], a[88:91], v[64:67]\n\t"
    "buffer_load_dwordx4 v18, s[40:43], s53 offen offset:2048 sc1 lds\n\t"
    "v_mfma_f32_16x16x32_bf16 v[68:71], a[20:23], a[88:91], v[68:71]\n\t"
    "v_mfma_f32_16x16x32_bf16 v[68:71], a[4:7], a[72:75], v[68:71]\n\t"
    "v_mfma_f32_16x16x32_bf16 v[72:75], a[8:11], a[72:75], v[72:75]\n\t"
    "v_mfma_f32_16x16x32_bf16 v[72:75], a[24:27], a[88:91], v[72:75]\n\t"
    "v_mfma_f32_16x16x32_bf16 v[76:79], a[28:31], a[88:91], v[76:79]\n\t"
    "buffer_load_dwordx4 v18, s[40:43], s53 offen offset:3072 sc1 lds\n\t"
    "v_mfma_f32_16x16x32_bf16 v[76:79], a[12:15], a[72:75], v[76:79]\n\t"
    "s_add_u32 s55, s55, s56\n\t"
    "s_add_u32 s40, s58, s55\n\t"
    "v_mfma_f32_16x16x32_bf16 v[92:95], a[12:15], a[76:79], v[92:95]\n\t"
    "s_addc_u32 s41, s59, 0\n\t"
    "s_add_u32 s69, s69, s64\n\t"
    "v_mfma_f32_16x16x32_bf16 v[92:95], a[28:31], a[92:95], v[92:95]\n\t"
    "s_sub_u32 s68, s68, 1\n\t"
    "s_cmp_lg_u32 s68, 2\n\t"
    "v_mfma_f32_16x16x32_bf16 v[88:91], a[24:27], a[92:95], v[88:91]\n\t"
    "v_mfma_f32_16x16x32_bf16 v[88:91], a[8:11], a[76:79], v[88:91]\n\t"
    "v_mfma_f32_16x16x32_bf16 v[84:87], a[4:7], a[76:79], v[84:87]\n\t"
    "v_mfma_f32_16x16x32_bf16 v[84:87], a[20:23], a[92:95], v[84:87]\n\t"
    "s_cbranch_scc1 Lloop2%=\n\t"
    "Lout2%=:\n\t"
    "v_mfma_f32_16x16x32_bf16 v[80:83], a[16:19], a[92:95], v[80:83]\n\t"
    "s_waitcnt vmcnt(12) lgkmcnt(0)\n\t"
    "v_mfma_f32_16x16x32_bf16 v[80:83], a[0:3], a[76:79], v[80:83]\n\t"
    "s_barrier\n\t"
    "v_mfma_f32_16x16x32_bf16 v[96:99], a[0:3], a[96:99], v[96:99]\n\t"
    "ds_read_b128 a[192:195], v9 offset:0\n\t"
    "s_cmp_eq_u32 s73, 1\n\t"
    "v_mfma_f32_16x16x32_bf16 v[96:99], a[16:19], a[112:115], v[96:99]\n\t"
    "ds_read_b128 a[196:199], v9 offset:1024\n\t"
    "s_cselect_b32 s71, s76, s75\n\t"
    "v_mfma_f32_16x16x32_bf16 v[100:103], a[20:23], a[112:115], v[100:103]\n\t"
    "ds_read_b128 a[200:203], v9 offset:16384\n\t"
    "v_add_u32 v238, s71, v8\n\t"
    "v_mfma_f32_16x16x32_bf16 v[100:103], a[4:7], a[96:99], v[100:103]\n\t"
    "ds_read_b128 a[204:207], v9 offset:17408\n\t"
    "v_add_u32 v239, s71, v9\n\t"
    "v_mfma_f32_16x16x32_bf16 v[104:107], a[8:11], a[96:99], v[104:107]\n\t"
    "ds_read_b64 a[128:129], v12 offset:0\n\t"
    "v_add_u32 v240, s71, v10\n\t"
    "v_mfma_f32_16x16x32_bf16 v[104:107], a[24:27], a[112:115], v[104:107]\n\t"
    "ds_read_b64 a[130:131], v13 offset:0\n\t"
    "v_add_u32 v241, s71, v11\n\t"
    "v_mfma_f32_16x16x32_bf16 v[108:111], a[28:31], a[112:115], v[108:111]\n\t"
    "ds_read_b64 a[132:133], v12 offset:2048\n\t"
    "s_cmp_eq_u32 s74, 1\n\t"
    "v_mfma_f32_16x16x32_bf16 v[108:111], a[12:15], a[96:99], v[108:111]\n\t"
    "ds_read_b64 a[134:135], v13 offset:2048\n\t"
    "s_cselect_b32 s72, s78, s77\n\t"
    "v_mfma_f32_16x16x32_bf16 v[124:127], a[12:15], a[100:103], v[124:127]\n\t"
    "ds_read_b64 a[136:137], v12 offset:4096\n\t"
    "v_add_u32 v242, s72, v12\n\t"
    "v_mfma_f32_16x16x32_bf16 v[124:127], a[28:31], a[116:119], v[124:127]\n\t"
    "ds_read_b64 a[138:139], v13 offset:4096\n\t"
    "v_add_u32 v243, s72, v13\n\t"
    "v_mfma_f32_16x16x32_bf16 v[120:123], a[24:27], a[116:119], v[120:123]\n\t"
    "ds_read_b64 a[140:141], v12 offset:6144\n\t"
    "v_add_u32 v244, s72, v14\n\t"
    "v_mfma_f32_16x16x32_bf16 v[120:123], a[8:11], a[100:103], v[120:123]\n\t"
    "ds_read_b64 a[142:143], v13 offset:6144\n\t"
    "v_add_u32 v245, s72, v15\n\t"
    "v_mfma_f32_16x16x32_bf16 v[116:119], a[4:7], a[100:103], v[116:119]\n\t"
    "ds_read_b64 a[144:145], v14 offset:0\n\t"
    "s_mov_b32 s83, 32768\n\t"
    "v_mfma_f32_16x16x32_bf16 v[116:119], a[20:23], a[116:119], v[116:119]\n\t"
    "ds_read_b64 a[146:147], v15 offset:0\n\t"
    "v_xor_b32 v234, 16384, v24\n\t"
    "v_mfma_f32_16x16x32_bf16 v[112:115], a[16:19], a[116:119], v[112:115]\n\t"
    "ds_read_b64 a[148:149], v14 offset:2048\n\t"
    "v_xor_b32 v235, 98304, v24\n\t"
    "v_mfma_f32_16x16x32_bf16 v[112:115], a[0:3], a[100:103], v[112:115]\n\t"
    "ds_read_b64 a[150:151], v15 offset:2048\n\t"
    "v_xor_b32 v236, 180224, v24\n\t"
    "v_mfma_f32_16x16x32_bf16 v[128:131], a[0:3], a[104:107], v[128:131]\n\t"
    "ds_read_b64 a[152:153], v14 offset:4096\n\t"
    "v_mfma_f32_16x16x32_bf16 v[128:131], a[16:19], a[120:123], v[128:131]\n\t"
    "ds_read_b64 a[154:155], v15 offset:4096\n\t"
    "v_mfma_f32_16x16x32_bf16 v[132:135], a[20:23], a[120:123], v[132:135]\n\t"
    "ds_read_b64 a[156:157], v14 offset:6144\n\t"
    "v_mfma_f32_16x16x32_bf16 v[132:135], a[4:7], a[104:107], v[132:135]\n\t"
    "ds_read_b64 a[158:159], v15 offset:6144\n\t"
    "v_mfma_f32_16x16x32_bf16 v[136:139], a[8:11], a[104:107], v[136:139]\n\t"
    "v_mfma_f32_16x16x32_bf16 v[136:139], a[24:27], a[120:123], v[136:139]\n\t"
    "v_mfma_f32_16x16x32_bf16 v[140:143], a[28:31], a[120:123], v[140:143]\n\t"
    "v_mfma_f32_16x16x32_bf16 v[140:143], a[12:15], a[104:107], v[140:143]\n\t"
    "v_mfma_f32_16x16x32_bf16 v[156:159], a[12:15], a[108:111], v[156:159]\n\t"
    "v_mfma_f32_16x16x32_bf16 v[156:159], a[28:31], a[124:127], v[156:159]\n\t"
    "v_mfma_f32_16x16x32_bf16 v[152:155], a[24:27], a[124:127], v[152:155]\n\t"
    "v_mfma_f32_16x16x32_bf16 v[152:155], a[8:11], a[108:111], v[152:155]\n\t"
    "v_mfma_f32_16x16x32_bf16 v[148:151], a[4:7], a[108:111], v[148:151]\n\t"
    "v_mfma_f32_16x16x32_bf16 v[148:151], a[20:23], a[124:127], v[148:151]\n\t"
    "v_mfma_f32_16x16x32_bf16 v[144:147], a[16:19], a[124:127], v[144:147]\n\t"
    "v_mfma_f32_16x16x32_bf16 v[144:147], a[0:3], a[108:111], v[144:147]\n\t"
    "s_waitcnt vmcnt(0) lgkmcnt(0)\n\t"
    "s_barrier\n\t"
    "v_mfma_f32_16x16x32_bf16 v[64:67], a[128:131], a[192:195], v[64:67]\n\t"
    "ds_read_b128 a[208:211], v239 offset:0\n\t"
    "ds_read_b128 a[212:215], v239 offset:1024\n\t"
    "v_mfma_f32_16x16x32_bf16 v[64:67], a[144:147], a[200:203], v[64:67]\n\t"
    "ds_read_b128 a[216:219], v239 offset:16384\n\t"
    "ds_read_b128 a[220:223], v239 offset:17408\n\t"
    "v_mfma_f32_16x16x32_bf16 v[68:71], a[132:135], a[192:195], v[68:71]\n\t"
    "ds_read_b64 a[160:161], v242 offset:0\n\t"
    "ds_read_b64 a[162:163], v243 offset:0\n\t"
    "v_mfma_f32_16x16x32_bf16 v[68:71], a[148:151], a[200:203], v[68:71]\n\t"
    "ds_read_b64 a[164:165], v242 offset:2048\n\t"
    "ds_read_b64 a[166:167], v243 offset:2048\n\t"
    "v_mfma_f32_16x16x32_bf16 v[72:75], a[136:139], a[192:195], v[72:75]\n\t"
    "ds_read_b64 a[168:169], v242 offset:4096\n\t"
    "ds_read_b64 a[170:171], v243 offset:4096\n\t"
    "v_mfma_f32_16x16x32_bf16 v[72:75], a[152:155], a[200:203], v[72:75]\n\t"
    "ds_read_b64 a[172:173], v242 offset:6144\n\t"
    "ds_read_b64 a[174:175], v243 offset:6144\n\t"
    "v_mfma_f32_16x16x32_bf16 v[76:79], a[140:143], a[192:195], v[76:79]\n\t"
    "ds_read_b64 a[176:177], v244 offset:0\n\t"
    "ds_read_b64 a[178:179], v245 offset:0\n\t"
    "v_mfma_f32_16x16x32_bf16 v[76:79], a[156:159], a[200:203], v[76:79]\n\t"
    "ds_read_b64 a[180:181], v244 offset:2048\n\t"
    "ds_read_b64 a[182:183], v245 offset:2048\n\t"
    "v_mfma_f32_16x16x32_bf16 v[92:95], a[140:143], a[196:199], v[92:95]\n\t"
    "ds_read_b64 a[184:185], v244 offset:4096\n\t"
    "ds_read_b64 a[186:187], v245 offset:4096\n\t"
    "v_mfma_f32_16x16x32_bf16 v[92:95], a[156:159], a[204:207], v[92:95]\n\t"
    "ds_read_b64 a[188:189], v244 offset:6144\n\t"
    "ds_read_b64 a[190:191], v245 offset:6144\n\t"
    "v_mfma_f32_16x16x32_bf16 v[88:91], a[136:139], a[196:199], v[88:91]\n\t"
    "v_mfma_f32_16x16x32_bf16 v[88:91], a[152:155], a[204:207], v[88:91]\n\t"
    "v_mfma_f32_16x16x32_bf16 v[84:87], a[132:135], a[196:199], v[84:87]\n\t"
    "v_mfma_f32_16x16x32_bf16 v[84:87], a[148:151], a[204:207], v[84:87]\n\t"
    "v_mfma_f32_16x16x32_bf16 v[80:83], a[128:131], a[196:199], v[80:83]\n\t"
    "v_mfma_f32_16x16x32_bf16 v[80:83], a[144:147], a[204:207], v[80:83]\n\t"
    "s_waitcnt lgkmcnt(0)\n\t"
    "v_mfma_f32_16x16x32_bf16 v[64:67], a[160:163], a[208:211], v[64:67]\n\t"
    "ds_read_b128 a[224:227], v10 offset:0\n\t"
    "ds_read_b128 a[228:231], v10 offset:1024\n\t"
    "v_mfma_f32_16x16x32_bf16 v[64:67], a[176:179], a[216:219], v[64:67]\n\t"
    "ds_read_b128 a[232:235], v10 offset:16384\n\t"
    "ds_read_b128 a[236:239], v10 offset:17408\n\t"
    "v_mfma_f32_16x16x32_bf16 v[68:71], a[164:167], a[208:211], v[68:71]\n\t"
    "ds_read_b128 a[240:243], v240 offset:0\n\t"
    "ds_read_b128 a[244:247], v240 offset:1024\n\t"
    "v_mfma_f32_16x16x32_bf16 v[68:71], a[180:183], a[216:219], v[68:71]\n\t"
    "ds_read_b128 a[248:251], v240 offset:16384\n\t"
    "ds_read_b128 a[252:255], v240 offset:17408\n\t"
    "v_mfma_f32_16x16x32_bf16 v[72:75], a[168:171], a[208:211], v[72:75]\n\t"
    "buffer_load_dwordx4 v[168:171], v25, s[92:95], 0 offen offset:0\n\t"
    "v_mfma_f32_16x16x32_bf16 v[72:75], a[184:187], a[216:219], v[72:75]\n\t"
    "v_mfma_f32_16x16x32_bf16 v[76:79], a[172:175], a[208:211], v[76:79]\n\t"
    "v_mfma_f32_16x16x32_bf16 v[76:79], a[188:191], a[216:219], v[76:79]\n\t"
    "buffer_load_dwordx4 v[172:175], v25, s[92:95], 0 offen offset:64\n\t"
    "v_mfma_f32_16x16x32_bf16 v[92:95], a[172:175], a[212:215], v[92:95]\n\t"
    "v_mfma_f32_16x16x32_bf16 v[92:95], a[188:191], a[220:223], v[92:95]\n\t"
    "v_mfma_f32_16x16x32_bf16 v[88:91], a[168:171], a[212:215], v[88:91]\n\t"
    "buffer_load_dwordx4 v[176:179], v25, s[92:95], s83 offen offset:0\n\t"
    "v_mfma_f32_16x16x32_bf16 v[88:91], a[184:187], a[220:223], v[88:91]\n\t"
    "v_mfma_f32_16x16x32_bf16 v[84:87], a[164:167], a[212:215], v[84:87]\n\t"
    "v_mfma_f32_16x16x32_bf16 v[84:87], a[180:183], a[220:223], v[84:87]\n\t"
    "buffer_load_dwordx4 v[180:183], v25, s[92:95], s83 offen offset:64\n\t"
    "v_mfma_f32_16x16x32_bf16 v[80:83], a[160:163], a[212:215], v[80:83]\n\t"
    "v_mfma_f32_16x16x32_bf16 v[80:83], a[176:179], a[220:223], v[80:83]\n\t"
    "s_waitcnt lgkmcnt(0)\n\t"
    "v_mfma_f32_16x16x32_bf16 v[96:99], a[128:131], a[224:227], v[96:99]\n\t"
    "ds_read_b128 a[192:195], v11 offset:0\n\t"
    "v_cvt_pk_bf16_f32 v64, v64, v65\n\t"
    "v_mfma_f32_16x16x32_bf16 v[96:99], a[144:147], a[232:235], v[96:99]\n\t"
    "v_cvt_pk_bf16_f32 v65, v66, v67\n\t"
    "v_cvt_pk_bf16_f32 v66, v68, v69\n\t"
    "v_mfma_f32_16x16x32_bf16 v[96:99], a[160:163], a[240:243], v[96:99]\n\t"
    "ds_read_b128 a[196:199], v11 offset:1024\n\t"
    "v_cvt_pk_bf16_f32 v67, v70, v71\n\t"
    "v_mfma_f32_16x16x32_bf16 v[96:99], a[176:179], a[248:251], v[96:99]\n\t"
    "buffer_store_dwordx4 v[64:67], v234, s[88:91], 0 offen offset:0 sc1\n\t"
    "v_cvt_pk_bf16_f32 v72, v72, v73\n\t"
    "v_mfma_f32_16x16x32_bf16 v[100:103], a[132:135], a[224:227], v[100:103]\n\t"
    "ds_read_b128 a[200:203], v11 offset:16384\n\t"
    "v_cvt_pk_bf16_f32 v73, v74, v75\n\t"
    "v_mfma_f32_16x16x32_bf16 v[100:103], a[148:151], a[232:235], v[100:103]\n\t"
    "v_cvt_pk_bf16_f32 v74, v76, v77\n\t"
    "v_cvt_pk_bf16_f32 v75, v78, v79\n\t"
    "v_mfma_f32_16x16x32_bf16 v[100:103], a[164:167], a[240:243], v[100:103]\n\t"
    "ds_read_b128 a[204:207], v11 offset:17408\n\t"
    "buffer_store_dwordx4 v[72:75], v234, s[88:91], 0 offen offset:1024 sc1\n\t"
    "v_mfma_f32_16x16x32_bf16 v[100:103], a[180:183], a[248:251], v[100:103]\n\t"
    "v_cvt_pk_bf16_f32 v80, v80, v81\n\t"
    "v_cvt_pk_bf16_f32 v81, v82, v83\n\t"
    "v_mfma_f32_16x16x32_bf16 v[104:107], a[136:139], a[224:227], v[104:107]\n\t"
    "ds_read_b128 a[208:211], v241 offset:0\n\t"
    "v_cvt_pk_bf16_f32 v82, v84, v85\n\t"
    "v_mfma_f32_16x16x32_bf16 v[104:107], a[152:155], a[232:235], v[104:107]\n\t"
    "v_cvt_pk_bf16_f32 v83, v86, v87\n\t"
    "buffer_store_dwordx4 v[80:83], v234, s[88:91], 0 offen offset:2048 sc1\n\t"
    "v_mfma_f32_16x16x32_bf16 v[104:107], a[168:171], a[240:243], v[104:107]\n\t"
    "ds_read_b128 a[212:215], v241 offset:1024\n\t"
    "v_cvt_pk_bf16_f32 v88, v88, v89\n\t"
    "v_mfma_f32_16x16x32_bf16 v[104:107], a[184:187], a[248:251], v[104:107]\n\t"
    "v_cvt_pk_bf16_f32 v89, v90, v91\n\t"
    "v_cvt_pk_bf16_f32 v90, v92, v93\n\t"
    "v_mfma_f32_16x16x32_bf16 v[108:111], a[140:143], a[224:227], v[108:111]\n\t"
    "ds_read_b128 a[216:219], v241 offset:16384\n\t"
    "v_cvt_pk_bf16_f32 v91, v94, v95\n\t"
    "v_mfma_f32_16x16x32_bf16 v[108:111], a[156:159], a[232:235], v[108:111]\n\t"
    "buffer_store_dwordx4 v[88:91], v234, s[88:91], 0 offen offset:3072 sc1\n\t"
    "s_mov_b32 s33, 0x00003f80\n\t"
    "v_mfma_f32_16x16x32_bf16 v[108:111], a[172:175], a[240:243], v[108:111]\n\t"
    "ds_read_b128 a[220:223], v241 offset:17408\n\t"
    "s_mov_b32 s34, 0x3f800000\n\t"
    "v_mfma_f32_16x16x32_bf16 v[108:111], a[188:191], a[248:251], v[108:111]\n\t"
    "s_and_b32 s31, %[b], 1\n\t"
    "s_mul_i32 s31, s31, 0\n\t"
    "v_mfma_f32_16x16x32_bf16 v[124:127], a[140:143], a[228:231], v[124:127]\n\t"
    "s_xor_b32 s31, s31, %[b]\n\t"
    "s_lshl_b32 s31, s31, 7\n\t"
    "v_mfma_f32_16x16x32_bf16 v[124:127], a[156:159], a[236:239], v[124:127]\n\t"
    "s_lshl_b32 s98, s22, 2\n\t"
    "s_add_u32 s31, s31, s98\n\t"
    "v_mfma_f32_16x16x32_bf16 v[124:127], a[172:175], a[244:247], v[124:127]\n\t"
    "s_add_u32 s31, s31, 32768\n\t"
    "v_min_u32 v3, 2, v1\n\t"
    "v_mfma_f32_16x16x32_bf16 v[124:127], a[188:191], a[252:255], v[124:127]\n\t"
    "v_lshl_add_u32 v29, v3, 4, s31\n\t"
    "v_add_u32 v3, 1, v3\n\t"
    "v_mfma_f32_16x16x32_bf16 v[120:123], a[136:139], a[228:231], v[120:123]\n\t"
    "v_lshlrev_b32 v3, 7, v3\n\t"
    "v_xor_b32 v6, v3, v29\n\t"
    "v_mfma_f32_16x16x32_bf16 v[120:123], a[152:155], a[236:239], v[120:123]\n\t"
    "v_mov_b32 v7, s27\n\t"
    "s_mov_b32 s82, 65536\n\t"
    "v_mfma_f32_16x16x32_bf16 v[120:123], a[168:171], a[244:247], v[120:123]\n\t"
    "s_mov_b32 s81, 131072\n\t"
    "v_mfma_f32_16x16x32_bf16 v[120:123], a[184:187], a[252:255], v[120:123]\n\t"
    "v_mfma_f32_16x16x32_bf16 v[116:119], a[132:135], a[228:231], v[116:119]\n\t"
    "v_mfma_f32_16x16x32_bf16 v[116:119], a[148:151], a[236:239], v[116:119]\n\t"
    "v_mfma_f32_16x16x32_bf16 v[116:119], a[164:167], a[244:247], v[116:119]\n\t"
    "v_mfma_f32_16x16x32_bf16 v[116:119], a[180:183], a[252:255], v[116:119]\n\t"
    "v_mfma_f32_16x16x32_bf16 v[112:115], a[128:131], a[228:231], v[112:115]\n\t"
    "v_mfma_f32_16x16x32_bf16 v[112:115], a[144:147], a[236:239], v[112:115]\n\t"
    "v_mfma_f32_16x16x32_bf16 v[112:115], a[160:163], a[244:247], v[112:115]\n\t"
    "v_mfma_f32_16x16x32_bf16 v[112:115], a[176:179], a[252:255], v[112:115]\n\t"
    "s_waitcnt lgkmcnt(0)\n\t"
    "v_mfma_f32_16x16x32_bf16 v[128:131], a[128:131], a[192:195], v[128:131]\n\t"
    "ds_read_b128 a[224:227], v8 offset:0\n\t"
    "v_cvt_pk_bf16_f32 v96, v96, v97\n\t"
    "v_mfma_f32_16x16x32_bf16 v[128:131], a[144:147], a[200:203], v[128:131]\n\t"
    "v_cvt_pk_bf16_f32 v97, v98, v99\n\t"
    "v_cvt_pk_bf16_f32 v98, v100, v101\n\t"
    "v_mfma_f32_16x16x32_bf16 v[128:131], a[160:163], a[208:211], v[128:131]\n\t"
    "ds_read_b128 a[228:231], v8 offset:1024\n\t"
    "v_cvt_pk_bf16_f32 v99, v102, v103\n\t"
    "v_mfma_f32_16x16x32_bf16 v[128:131], a[176:179], a[216:219], v[128:131]\n\t"
    "buffer_store_dwordx4 v[96:99], v235, s[88:91], 0 offen offset:0 sc1\n\t"
    "v_cvt_pk_bf16_f32 v104, v104, v105\n\t"
    "v_mfma_f32_16x16x32_bf16 v[132:135], a[132:135], a[192:195], v[132:135]\n\t"
    "ds_read_b128 a[232:235], v8 offset:16384\n\t"
    "v_cvt_pk_bf16_f32 v105, v106, v107\n\t"
    "v_mfma_f32_16x16x32_bf16 v[132:135], a[148:151], a[200:203], v[132:135]\n\t"
    "v_cvt_pk_bf16_f32 v106, v108, v109\n\t"
    "v_cvt_pk_bf16_f32 v107, v110, v111\n\t"
    "v_mfma_f32_16x16x32_bf16 v[132:135], a[164:167], a[208:211], v[132:135]\n\t"
    "ds_read_b128 a[236:239], v8 offset:17408\n\t"
    "buffer_store_dwordx4 v[104:107], v235, s[88:91], 0 offen offset:1024 sc1\n\t"
    "v_mfma_f32_16x16x32_bf16 v[132:135], a[180:183], a[216:219], v[132:135]\n\t"
    "v_cvt_pk_bf16_f32 v112, v112, v113\n\t"
    "v_cvt_pk_bf16_f32 v113, v114, v115\n\t"
    "v_mfma_f32_16x16x32_bf16 v[136:139], a[136:139], a[192:195], v[136:139]\n\t"
    "ds_read_b128 a[240:243], v238 offset:0\n\t"
    "v_cvt_pk_bf16_f32 v114, v116, v117\n\t"
    "v_mfma_f32_16x16x32_bf16 v[136:139], a[152:155], a[200:203], v[136:139]\n\t"
    "v_cvt_pk_bf16_f32 v115, v118, v119\n\t"
    "buffer_store_dwordx4 v[112:115], v235, s[88:91], 0 offen offset:2048 sc1\n\t"
    "v_mfma_f32_16x16x32_bf16 v[136:139], a[168:171], a[208:211], v[136:139]\n\t"
    "ds_read_b128 a[244:247], v238 offset:1024\n\t"
    "v_cvt_pk_bf16_f32 v120, v120, v121\n\t"
    "v_mfma_f32_16x16x32_bf16 v[136:139], a[184:187], a[216:219], v[136:139]\n\t"
    "v_cvt_pk_bf16_f32 v121, v122, v123\n\t"
    "v_cvt_pk_bf16_f32 v122, v124, v125\n\t"
    "v_mfma_f32_16x16x32_bf16 v[140:143], a[140:143], a[192:195], v[140:143]\n\t"
    "ds_read_b128 a[248:251], v238 offset:16384\n\t"
    "v_cvt_pk_bf16_f32 v123, v126, v127\n\t"
    "v_mfma_f32_16x16x32_bf16 v[140:143], a[156:159], a[200:203], v[140:143]\n\t"
    "buffer_store_dwordx4 v[120:123], v235, s[88:91], 0 offen offset:3072 sc1\n\t"
    "v_mfma_f32_16x16x32_bf16 v[140:143], a[172:175], a[208:211], v[140:143]\n\t"
    "ds_read_b128 a[252:255], v238 offset:17408\n\t"
    "v_mfma_f32_16x16x32_bf16 v[140:143], a[188:191], a[216:219], v[140:143]\n\t"
    "v_mfma_f32_16x16x32_bf16 v[156:159], a[140:143], a[196:199], v[156:159]\n\t"
    "v_mfma_f32_16x16x32_bf16 v[156:159], a[156:159], a[204:207], v[156:159]\n\t"
    "v_mfma_f32_16x16x32_bf16 v[156:159], a[172:175], a[212:215], v[156:159]\n\t"
    "v_mfma_f32_16x16x32_bf16 v[156:159], a[188:191], a[220:223], v[156:159]\n\t"
    "v_mfma_f32_16x16x32_bf16 v[152:155], a[136:139], a[196:199], v[152:155]\n\t"
    "v_mfma_f32_16x16x32_bf16 v[152:155], a[152:155], a[204:207], v[152:155]\n\t"
    "v_mfma_f32_16x16x32_bf16 v[152:155], a[168:171], a[212:215], v[152:155]\n\t"
    "v_mfma_f32_16x16x32_bf16 v[152:155], a[184:187], a[220:223], v[152:155]\n\t"
    "v_mfma_f32_16x16x32_bf16 v[148:151], a[132:135], a[196:199], v[148:151]\n\t"
    "v_mfma_f32_16x16x32_bf16 v[148:151], a[148:151], a[204:207], v[148:151]\n\t"
    "v_mfma_f32_16x16x32_bf16 v[148:151], a[164:167], a[212:215], v[148:151]\n\t"
    "v_mfma_f32_16x16x32_bf16 v[148:151], a[180:183], a[220:223], v[148:151]\n\t"
    "v_mfma_f32_16x16x32_bf16 v[144:147], a[128:131], a[196:199], v[144:147]\n\t"
    "v_mfma_f32_16x16x32_bf16 v[144:147], a[144:147], a[204:207], v[144:147]\n\t"
    "v_mfma_f32_16x16x32_bf16 v[144:147], a[160:163], a[212:215], v[144:147]\n\t"
    "v_mfma_f32_16x16x32_bf16 v[144:147], a[176:179], a[220:223], v[144:147]\n\t"
    "s_waitcnt lgkmcnt(0)\n\t"
    "v_mfma_f32_16x16x32_bf16 v[32:35], a[128:131], a[224:227], v[32:35]\n\t"
    "v_cvt_pk_bf16_f32 v128, v128, v129\n\t"
    "v_cvt_pk_bf16_f32 v129, v130, v131\n\t"
    "v_mfma_f32_16x16x32_bf16 v[32:35], a[144:147], a[232:235], v[32:35]\n\t"
    "v_cvt_pk_bf16_f32 v130, v132, v133\n\t"
    "v_cvt_pk_bf16_f32 v131, v134, v135\n\t"
    "v_mfma_f32_16x16x32_bf16 v[32:35], a[160:163], a[240:243], v[32:35]\n\t"
    "buffer_store_dwordx4 v[128:131], v236, s[88:91], 0 offen offset:0 sc1\n\t"
    "v_cvt_pk_bf16_f32 v136, v136, v137\n\t"
    "v_mfma_f32_16x16x32_bf16 v[32:35], a[176:179], a[248:251], v[32:35]\n\t"
    "v_cvt_pk_bf16_f32 v137, v138, v139\n\t"
    "v_cvt_pk_bf16_f32 v138, v140, v141\n\t"
    "v_mfma_f32_16x16x32_bf16 v[36:39], a[132:135], a[224:227], v[36:39]\n\t"
    "v_cvt_pk_bf16_f32 v139, v142, v143\n\t"
    "buffer_store_dwordx4 v[136:139], v236, s[88:91], 0 offen offset:1024 sc1\n\t"
    "v_mfma_f32_16x16x32_bf16 v[36:39], a[148:151], a[232:235], v[36:39]\n\t"
    "v_cvt_pk_bf16_f32 v144, v144, v145\n\t"
    "v_cvt_pk_bf16_f32 v145, v146, v147\n\t"
    "v_mfma_f32_16x16x32_bf16 v[36:39], a[164:167], a[240:243], v[36:39]\n\t"
    "v_cvt_pk_bf16_f32 v146, v148, v149\n\t"
    "v_cvt_pk_bf16_f32 v147, v150, v151\n\t"
    "v_mfma_f32_16x16x32_bf16 v[36:39], a[180:183], a[248:251], v[36:39]\n\t"
    "buffer_store_dwordx4 v[144:147], v236, s[88:91], 0 offen offset:2048 sc1\n\t"
    "v_cvt_pk_bf16_f32 v152, v152, v153\n\t"
    "v_mfma_f32_16x16x32_bf16 v[40:43], a[136:139], a[224:227], v[40:43]\n\t"
    "v_cvt_pk_bf16_f32 v153, v154, v155\n\t"
    "v_cvt_pk_bf16_f32 v154, v156, v157\n\t"
    "v_mfma_f32_16x16x32_bf16 v[40:43], a[152:155], a[232:235], v[40:43]\n\t"
    "v_cvt_pk_bf16_f32 v155, v158, v159\n\t"
    "buffer_store_dwordx4 v[152:155], v236, s[88:91], 0 offen offset:3072 sc1\n\t"
    "v_mfma_f32_16x16x32_bf16 v[40:43], a[168:171], a[240:243], v[40:43]\n\t"
    "v_mfma_f32_16x16x32_bf16 v[40:43], a[184:187], a[248:251], v[40:43]\n\t"
    "v_mfma_f32_16x16x32_bf16 v[44:47], a[140:143], a[224:227], v[44:47]\n\t"
    "v_mfma_f32_16x16x32_bf16 v[44:47], a[156:159], a[232:235], v[44:47]\n\t"
    "v_mfma_f32_16x16x32_bf16 v[44:47], a[172:175], a[240:243], v[44:47]\n\t"
    "v_mfma_f32_16x16x32_bf16 v[44:47], a[188:191], a[248:251], v[44:47]\n\t"
    "v_mfma_f32_16x16x32_bf16 v[60:63], a[140:143], a[228:231], v[60:63]\n\t"
    "v_mfma_f32_16x16x32_bf16 v[60:63], a[156:159], a[236:239], v[60:63]\n\t"
    "v_mfma_f32_16x16x32_bf16 v[60:63], a[172:175], a[244:247], v[60:63]\n\t"
    "v_mfma_f32_16x16x32_bf16 v[60:63], a[188:191], a[252:255], v[60:63]\n\t"
    "v_mfma_f32_16x16x32_bf16 v[56:59], a[136:139], a[228:231], v[56:59]\n\t"
    "v_mfma_f32_16x16x32_bf16 v[56:59], a[152:155], a[236:239], v[56:59]\n\t"
    "v_mfma_f32_16x16x32_bf16 v[56:59], a[168:171], a[244:247], v[56:59]\n\t"
    "v_mfma_f32_16x16x32_bf16 v[56:59], a[184:187], a[252:255], v[56:59]\n\t"
    "v_mfma_f32_16x16x32_bf16 v[52:55], a[132:135], a[228:231], v[52:55]\n\t"
    "v_mfma_f32_16x16x32_bf16 v[52:55], a[148:151], a[236:239], v[52:55]\n\t"
    "v_mfma_f32_16x16x32_bf16 v[52:55], a[164:167], a[244:247], v[52:55]\n\t"
    "v_mfma_f32_16x16x32_bf16 v[52:55], a[180:183], a[252:255], v[52:55]\n\t"
    "v_mfma_f32_16x16x32_bf16 v[48:51], a[128:131], a[228:231], v[48:51]\n\t"
    "v_mfma_f32_16x16x32_bf16 v[48:51], a[144:147], a[236:239], v[48:51]\n\t"
    "v_mfma_f32_16x16x32_bf16 v[48:51], a[160:163], a[244:247], v[48:51]\n\t"
    "v_mfma_f32_16x16x32_bf16 v[48:51], a[176:179], a[252:255], v[48:51]\n\t"
    "s_waitcnt vmcnt(8)\n\t"
    "s_mov_b64 exec, 1\n\t"
    "global_store_dword v6, v7, s[28:29] sc1\n\t"
    "s_mov_b64 exec, -1\n\t"
    "v_permlane16_swap_b32 v168, v170\n\t"
    "v_permlane16_swap_b32 v169, v171\n\t"
    "v_permlane16_swap_b32 v172, v174\n\t"
    "v_permlane16_swap_b32 v173, v175\n\t"
    "v_permlane16_swap_b32 v176, v178\n\t"
    "v_permlane16_swap_b32 v177, v179\n\t"
    "v_permlane16_swap_b32 v180, v182\n\t"
    "v_permlane16_swap_b32 v181, v183\n\t"
    "v_dot2_f32_bf16 v32, v168, s33, v32\n\t"
    "v_dot2_f32_bf16 v33, v168, s34, v33\n\t"
    "v_dot2_f32_bf16 v34, v169, s33, v34\n\t"
    "v_dot2_f32_bf16 v35, v169, s34, v35\n\t"
    "v_dot2_f32_bf16 v36, v170, s33, v36\n\t"
    "v_dot2_f32_bf16 v37, v170, s34, v37\n\t"
    "v_dot2_f32_bf16 v38, v171, s33, v38\n\t"
    "v_dot2_f32_bf16 v39, v171, s34, v39\n\t"
    "v_dot2_f32_bf16 v40, v172, s33, v40\n\t"
    "v_dot2_f32_bf16 v41, v172, s34, v41\n\t"
    "v_dot2_f32_bf16 v42, v173, s33, v42\n\t"
    "v_dot2_f32_bf16 v43, v173, s34, v43\n\t"
    "v_dot2_f32_bf16 v44, v174, s33, v44\n\t"
    "v_dot2_f32_bf16 v45, v174, s34, v45\n\t"
    "v_dot2_f32_bf16 v46, v175, s33, v46\n\t"
    "v_dot2_f32_bf16 v47, v175, s34, v47\n\t"
    "v_dot2_f32_bf16 v48, v176, s33, v48\n\t"
    "v_dot2_f32_bf16 v49, v176, s34, v49\n\t"
    "v_dot2_f32_bf16 v50, v177, s33, v50\n\t"
    "v_dot2_f32_bf16 v51, v177, s34, v51\n\t"
    "v_dot2_f32_bf16 v52, v178, s33, v52\n\t"
    "v_dot2_f32_bf16 v53, v178, s34, v53\n\t"
    "v_dot2_f32_bf16 v54, v179, s33, v54\n\t"
    "v_dot2_f32_bf16 v55, v179, s34, v55\n\t"
    "v_dot2_f32_bf16 v56, v180, s33, v56\n\t"
    "v_dot2_f32_bf16 v57, v180, s34, v57\n\t"
    "v_dot2_f32_bf16 v58, v181, s33, v58\n\t"
    "v_dot2_f32_bf16 v59, v181, s34, v59\n\t"
    "v_dot2_f32_bf16 v60, v182, s33, v60\n\t"
    "v_dot2_f32_bf16 v61, v182, s34, v61\n\t"
    "v_dot2_f32_bf16 v62, v183, s33, v62\n\t"
    "v_dot2_f32_bf16 v63, v183, s34, v63\n\t"
    "s_waitcnt vmcnt(5)\n\t"
    "s_mov_b64 exec, 2\n\t"
    "global_store_dword v6, v7, s[28:29] sc1\n\t"
    "s_mov_b64 exec, -1\n\t"
    "s_waitcnt vmcnt(2)\n\t"
    "s_mov_b64 exec, 4\n\t"
    "global_store_dword v6, v7, s[28:29] sc1\n\t"
    "s_mov_b64 exec, -1\n\t"
    "s_lshl_b32 s98, %[b], 2\n\t"
    "s_add_u32 s98, s98, s22\n\t"
    "s_cmp_lg_u32 s98, 1023\n\t"
    "s_cbranch_scc1 Lk1%=\n\t"
    "v_lshrrev_b32 v3, 3, v1\n\t"
    "v_lshlrev_b32 v3, 8, v3\n\t"
    "v_and_b32 v4, 7, v0\n\t"
    "v_lshl_add_u32 v3, v4, 2, v3\n\t"
    "v_add_u32 v5, 16384, v3\n\t"
    "global_load_dword v232, v5, s[28:29] sc1\n\t"
    "global_load_dword v233, v5, s[28:29] offset:2048 sc1\n\t"
    "Lk1%=:\n\t"
    "s_mov_b32 s31, 0\n\t"
    "s_mov_b32 s84, 0x20000\n\t"
    "Lq0%=:\n\t"
    "global_load_dword v28, v29, s[28:29] sc1\n\t"
    "s_waitcnt vmcnt(0)\n\t"
    "v_cmp_eq_u32 vcc, s27, v28\n\t"
    "s_sub_u32 s84, s84, 1\n\t"
    "s_cselect_b32 s98, 7, 0\n\t"
    "s_nop 1\n\t"
    "s_or_b32 s98, s98, vcc_lo\n\t"
    "s_and_b32 s98, s98, 7\n\t"
    "s_andn2_b32 s98, s98, s31\n\t"
    "s_or_b32 s31, s31, s98\n\t"
    "s_bitcmp0_b32 s98, 0\n\t"
    "s_cbranch_scc1 Lq10%=\n\t"
    "buffer_load_dwordx4 v[184:187], v24, s[88:91], 0 offen offset:0 sc1\n\t"
    "buffer_load_dwordx4 v[188:191], v24, s[88:91], 0 offen offset:1024 sc1\n\t"
    "buffer_load_dwordx4 v[192:195], v24, s[88:91], 0 offen offset:2048 sc1\n\t"
    "buffer_load_dwordx4 v[196:199], v24, s[88:91], 0 offen offset:3072 sc1\n\t"
    "Lq10%=:\n\t"
    "s_bitcmp0_b32 s98, 1\n\t"
    "s_cbranch_scc1 Lq11%=\n\t"
    "buffer_load_dwordx4 v[200:203], v24, s[88:91], s82 offen offset:0 sc1\n\t"
    "buffer_load_dwordx4 v[204:207], v24, s[88:91], s82 offen offset:1024 sc1\n\t"
    "buffer_load_dwordx4 v[208:211], v24, s[88:91], s82 offen offset:2048 sc1\n\t"
    "buffer_load_dwordx4 v[212:215], v24, s[88:91], s82 offen offset:3072 sc1\n\t"
    "Lq11%=:\n\t"
    "s_bitcmp0_b32 s98, 2\n\t"
    "s_cbranch_scc1 Lq12%=\n\t"
    "buffer_load_dwordx4 v[216:219], v24, s[88:91], s81 offen offset:0 sc1\n\t"
    "buffer_load_dwordx4 v[220:223], v24, s[88:91], s81 offen offset:1024 sc1\n\t"
    "buffer_load_dwordx4 v[224:227], v24, s[88:91], s81 offen offset:2048 sc1\n\t"
    "buffer_load_dwordx4 v[228:231], v24, s[88:91], s81 offen offset:3072 sc1\n\t"
    "Lq12%=:\n\t"
    "s_cmp_eq_u32 s31, 7\n\t"
    "s_cbranch_scc1 Lq9%=\n\t"
    "s_sleep 0\n\t"
    "s_branch Lq0%=\n\t"
    "Lq9%=:\n\t"
    "s_waitcnt vmcnt(0)\n\t"
    "v_dot2_f32_bf16 v32, v184, s33, v32\n\t"
    "v_dot2_f32_bf16 v33, v184, s34, v33\n\t"
    "v_dot2_f32_bf16 v34, v185, s33, v34\n\t"
    "v_dot2_f32_bf16 v35, v185, s34, v35\n\t"
    "v_dot2_f32_bf16 v36, v186, s33, v36\n\t"
    "v_dot2_f32_bf16 v37, v186, s34, v37\n\t"
    "v_dot2_f32_bf16 v38, v187, s33, v38\n\t"
    "v_dot2_f32_bf16 v39, v187, s34, v39\n\t"
    "v_dot2_f32_bf16 v32, v200, s33, v32\n\t"
    "v_dot2_f32_bf16 v33, v200, s34, v33\n\t"
    "v_dot2_f32_bf16 v34, v201, s33, v34\n\t"
    "v_dot2_f32_bf16 v35, v201, s34, v35\n\t"
    "v_dot2_f32_bf16 v36, v202, s33, v36\n\t"
    "v_dot2_f32_bf16 v37, v202, s34, v37\n\t"
    "v_dot2_f32_bf16 v38, v203, s33, v38\n\t"
    "v_dot2_f32_bf16 v39, v203, s34, v39\n\t"
    "v_dot2_f32_bf16 v32, v216, s33, v32\n\t"
    "v_dot2_f32_bf16 v33, v216, s34, v33\n\t"
    "v_dot2_f32_bf16 v34, v217, s33, v34\n\t"
    "v_dot2_f32_bf16 v35, v217, s34, v35\n\t"
    "v_dot2_f32_bf16 v36, v218, s33, v36\n\t"
    "v_dot2_f32_bf16 v37, v218, s34, v37\n\t"
    "v_dot2_f32_bf16 v38, v219, s33, v38\n\t"
    "v_dot2_f32_bf16 v39, v219, s34, v39\n\t"
    "s_nop 2\n\t"
    "v_cvt_pk_bf16_f32 v32, v32, v33\n\t"
    "v_cvt_pk_bf16_f32 v33, v34, v35\n\t"
    "v_cvt_pk_bf16_f32 v34, v36, v37\n\t"
    "v_cvt_pk_bf16_f32 v35, v38, v39\n\t"
    "s_nop 1\n\t"
    "v_permlane16_swap_b32 v32, v34\n\t"
    "v_permlane16_swap_b32 v33, v35\n\t"
    "buffer_store_dwordx4 v[32:35], v25, s[48:51], 0 offen offset:0 sc1\n\t"
    "v_dot2_f32_bf16 v40, v188, s33, v40\n\t"
    "v_dot2_f32_bf16 v41, v188, s34, v41\n\t"
    "v_dot2_f32_bf16 v42, v189, s33, v42\n\t"
    "v_dot2_f32_bf16 v43, v189, s34, v43\n\t"
    "v_dot2_f32_bf16 v44, v190, s33, v44\n\t"
    "v_dot2_f32_bf16 v45, v190, s34, v45\n\t"
    "v_dot2_f32_bf16 v46, v191, s33, v46\n\t"
    "v_dot2_f32_bf16 v47, v191, s34, v47\n\t"
    "v_dot2_f32_bf16 v40, v204, s33, v40\n\t"
    "v_dot2_f32_bf16 v41, v204, s34, v41\n\t"
    "v_dot2_f32_bf16 v42, v205, s33, v42\n\t"
    "v_dot2_f32_bf16 v43, v205, s34, v43\n\t"
    "v_dot2_f32_bf16 v44, v206, s33, v44\n\t"
    "v_dot2_f32_bf16 v45, v206, s34, v45\n\t"
    "v_dot2_f32_bf16 v46, v207, s33, v46\n\t"
    "v_dot2_f32_bf16 v47, v207, s34, v47\n\t"
    "v_dot2_f32_bf16 v40, v220, s33, v40\n\t"
    "v_dot2_f32_bf16 v41, v220, s34, v41\n\t"
    "v_dot2_f32_bf16 v42, v221, s33, v42\n\t"
    "v_dot2_f32_bf16 v43, v221, s34, v43\n\t"
    "v_dot2_f32_bf16 v44, v222, s33, v44\n\t"
    "v_dot2_f32_bf16 v45, v222, s34, v45\n\t"
    "v_dot2_f32_bf16 v46, v223, s33, v46\n\t"
    "v_dot2_f32_bf16 v47, v223, s34, v47\n\t"
    "s_nop 2\n\t"
    "v_cvt_pk_bf16_f32 v40, v40, v41\n\t"
    "v_cvt_pk_bf16_f32 v41, v42, v43\n\t"
    "v_cvt_pk_bf16_f32 v42, v44, v45\n\t"
    "v_cvt_pk_bf16_f32 v43, v46, v47\n\t"
    "s_nop 1\n\t"
    "v_permlane16_swap_b32 v40, v42\n\t"
    "v_permlane16_swap_b32 v41, v43\n\t"
    "buffer_store_dwordx4 v[40:43], v25, s[48:51], 0 offen offset:64 sc1\n\t"
    "v_dot2_f32_bf16 v48, v192, s33, v48\n\t"
    "v_dot2_f32_bf16 v49, v192, s34, v49\n\t"
    "v_dot2_f32_bf16 v50, v193, s33, v50\n\t"
    "v_dot2_f32_bf16 v51, v193, s34, v51\n\t"
    "v_dot2_f32_bf16 v52, v194, s33, v52\n\t"
    "v_dot2_f32_bf16 v53, v194, s34, v53\n\t"
    "v_dot2_f32_bf16 v54, v195, s33, v54\n\t"
    "v_dot2_f32_bf16 v55, v195, s34, v55\n\t"
    "v_dot2_f32_bf16 v48, v208, s33, v48\n\t"
    "v_dot2_f32_bf16 v49, v208, s34, v49\n\t"
    "v_dot2_f32_bf16 v50, v209, s33, v50\n\t"
    "v_dot2_f32_bf16 v51, v209, s34, v51\n\t"
    "v_dot2_f32_bf16 v52, v210, s33, v52\n\t"
    "v_dot2_f32_bf16 v53, v210, s34, v53\n\t"
    "v_dot2_f32_bf16 v54, v211, s33, v54\n\t"
    "v_dot2_f32_bf16 v55, v211, s34, v55\n\t"
    "v_dot2_f32_bf16 v48, v224, s33, v48\n\t"
    "v_dot2_f32_bf16 v49, v224, s34, v49\n\t"
    "v_dot2_f32_bf16 v50, v225, s33, v50\n\t"
    "v_dot2_f32_bf16 v51, v225, s34, v51\n\t"
    "v_dot2_f32_bf16 v52, v226, s33, v52\n\t"
    "v_dot2_f32_bf16 v53, v226, s34, v53\n\t"
    "v_dot2_f32_bf16 v54, v227, s33, v54\n\t"
    "v_dot2_f32_bf16 v55, v227, s34, v55\n\t"
    "s_nop 2\n\t"
    "v_cvt_pk_bf16_f32 v48, v48, v49\n\t"
    "v_cvt_pk_bf16_f32 v49, v50, v51\n\t"
    "v_cvt_pk_bf16_f32 v50, v52, v53\n\t"
    "v_cvt_pk_bf16_f32 v51, v54, v55\n\t"
    "s_nop 1\n\t"
    "v_permlane16_swap_b32 v48, v50\n\t"
    "v_permlane16_swap_b32 v49, v51\n\t"
    "buffer_store_dwordx4 v[48:51], v25, s[48:51], s83 offen offset:0 sc1\n\t"
    "v_dot2_f32_bf16 v56, v196, s33, v56\n\t"
    "v_dot2_f32_bf16 v57, v196, s34, v57\n\t"
    "v_dot2_f32_bf16 v58, v197, s33, v58\n\t"
    "v_dot2_f32_bf16 v59, v197, s34, v59\n\t"
    "v_dot2_f32_bf16 v60, v198, s33, v60\n\t"
    "v_dot2_f32_bf16 v61, v198, s34, v61\n\t"
    "v_dot2_f32_bf16 v62, v199, s33, v62\n\t"
    "v_dot2_f32_bf16 v63, v199, s34, v63\n\t"
    "v_dot2_f32_bf16 v56, v212, s33, v56\n\t"
    "v_dot2_f32_bf16 v57, v212, s34, v57\n\t"
    "v_dot2_f32_bf16 v58, v213, s33, v58\n\t"
    "v_dot2_f32_bf16 v59, v213, s34, v59\n\t"
    "v_dot2_f32_bf16 v60, v214, s33, v60\n\t"
    "v_dot2_f32_bf16 v61, v214, s34, v61\n\t"
    "v_dot2_f32_bf16 v62, v215, s33, v62\n\t"
    "v_dot2_f32_bf16 v63, v215, s34, v63\n\t"
    "v_dot2_f32_bf16 v56, v228, s33, v56\n\t"
    "v_dot2_f32_bf16 v57, v228, s34, v57\n\t"
    "v_dot2_f32_bf16 v58, v229, s33, v58\n\t"
    "v_dot2_f32_bf16 v59, v229, s34, v59\n\t"
    "v_dot2_f32_bf16 v60, v230, s33, v60\n\t"
    "v_dot2_f32_bf16 v61, v230, s34, v61\n\t"
    "v_dot2_f32_bf16 v62, v231, s33, v62\n\t"
    "v_dot2_f32_bf16 v63, v231, s34, v63\n\t"
    "s_nop 2\n\t"
    "v_cvt_pk_bf16_f32 v56, v56, v57\n\t"
    "v_cvt_pk_bf16_f32 v57, v58, v59\n\t"
    "v_cvt_pk_bf16_f32 v58, v60, v61\n\t"
    "v_cvt_pk_bf16_f32 v59, v62, v63\n\t"
    "s_nop 1\n\t"
    "v_permlane16_swap_b32 v56, v58\n\t"
    "v_permlane16_swap_b32 v57, v59\n\t"
    "buffer_store_dwordx4 v[56:59], v25, s[48:51], s83 offen offset:64 sc1\n\t"
    "s_lshl_b32 s98, %[b], 2\n\t"
    "s_add_u32 s98, s98, s22\n\t"
    "s_cmp_lg_u32 s98, 1023\n\t"
    "s_cbranch_scc1 Lok%=\n\t"
    "v_cmp_ne_u32 vcc, s27, v232\n\t"
    "v_cmp_ne_u32 s[96:97], s27, v233\n\t"
    "s_nop 1\n\t"
    "s_or_b64 vcc, vcc, s[96:97]\n\t"
    "s_nop 1\n\t"
    "s_cbranch_vccz Lsdk%=\n\t"
    "s_mov_b32 s84, 0x20000\n\t"
    "Lspk%=:\n\t"
    "global_load_dword v6, v5, s[28:29] offset:0 sc1\n\t"
    "global_load_dword v7, v5, s[28:29] offset:2048 sc1\n\t"
    "s_waitcnt vmcnt(0)\n\t"
    "v_cmp_ne_u32 vcc, s27, v6\n\t"
    "v_cmp_ne_u32 s[96:97], s27, v7\n\t"
    "s_nop 1\n\t"
    "s_or_b64 vcc, vcc, s[96:97]\n\t"
    "s_sub_u32 s84, s84, 1\n\t"
    "s_cbranch_scc1 Lsdk%=\n\t"
    "s_nop 2\n\t"
    "s_cbranch_vccz Lsdk%=\n\t"
    "s_sleep 8\n\t"
    "s_branch Lspk%=\n\t"
    "Lsdk%=:\n\t"
    "s_cmp_lg_u32 s22, 3\n\t"
    "s_cbranch_scc1 Lok%=\n\t"
    "v_mov_b32 v3, 69632\n\t"
    "v_mov_b32 v4, s27\n\t"
    "s_mov_b64 exec, 1\n\t"
    "global_store_dword v3, v4, s[28:29] sc1\n\t"
    "s_mov_b64 exec, -1\n\t"
    "Lok%=:\n\t"
    "Lend%=:\n\t"
    :: [ka]"s"(__builtin_amdgcn_kernarg_segment_ptr()), [b]"s"(blockIdx.x), "{v0}"(threadIdx.x), "s"((unsigned)(size_t)(LAS char*)smem)
    : "memory", "scc", "vcc", "s4", "s5", "s6", "s7", "s8", "s9", "s10", "s11", "s12", "s13", "s14", "s15", "s16", "s17", "s18", "s19", "s20", "s21", "s22", "s23", "s24", "s25", "s26", "s27", "s28", "s29", "s30", "s31", "s33", "s34", "s35", "s36", "s37", "s38", "s39", "s40", "s41", "s42", "s43", "s44", "s45", "s46", "s47", "s48", "s49", "s50", "s51", "s52", "s53", "s54", "s55", "s56", "s57", "s58", "s59", "s60", "s61", "s62", "s63", "s64", "s65", "s66", "s67", "s68", "s69", "s70", "s71", "s72", "s73", "s74", "s75", "s76", "s77", "s78", "s79", "s80", "s81", "s82", "s83", "s84", "s85", "s86", "s87", "s88", "s89", "s90", "s91", "s92", "s93", "s94", "s95", "s96", "s97", "s98", "v1", "v2", "v3", "v4", "v5", "v6", "v7", "v8", "v9", "v10", "v11", "v12", "v13", "v14", "v15", "v16", "v17", "v18", "v19", "v20", "v21", "v22", "v23", "v24", "v25", "v26", "v27", "v28", "v29", "v30", "v31", "v32", "v33", "v34", "v35", "v36", "v37", "v38", "v39", "v40", "v41", "v42", "v43", "v44", "v45", "v46", "v47", "v48", "v49", "v50", "v51", "v52", "v53", "v54", "v55", "v56", "v57", "v58", "v59", "v60", "v61", "v62", "v63", "v64", "v65", "v66", "v67", "v68", "v69", "v70", "v71", "v72", "v73", "v74", "v75", "v76", "v77", "v78", "v79", "v80", "v81", "v82", "v83", "v84", "v85", "v86", "v87", "v88", "v89", "v90", "v91", "v92", "v93", "v94", "v95", "v96", "v97", "v98", "v99", "v100", "v101", "v102", "v103", "v104", "v105", "v106", "v107", "v108", "v109", "v110", "v111", "v112", "v113", "v114", "v115", "v116", "v117", "v118", "v119", "v120", "v121", "v122", "v123", "v124", "v125", "v126", "v127", "v128", "v129", "v130", "v131", "v132", "v133", "v134", "v135", "v136", "v137", "v138", "v139", "v140", "v141", "v142", "v143", "v144", "v145", "v146", "v147", "v148", "v149", "v150", "v151", "v152", "v153", "v154", "v155", "v156", "v157", "v158", "v159", "v160", "v161", "v162", "v163", "v164", "v165", "v166", "v167", "v168", "v169", "v170", "v171", "v172", "v173", "v174", "v175", "v176", "v177", "v178", "v179", "v180", "v181", "v182", "v183", "v184", "v185", "v186", "v187", "v188", "v189", "v190", "v191", "v192", "v193", "v194", "v195", "v196", "v197", "v198", "v199", "v200", "v201", "v202", "v203", "v204", "v205", "v206", "v207", "v208", "v209", "v210", "v211", "v212", "v213", "v214", "v215", "v216", "v217", "v218", "v219", "v220", "v221", "v222", "v223", "v224", "v225", "v226", "v227", "v228", "v229", "v230", "v231", "v232", "v233", "v234", "v235", "v236", "v237", "v238", "v239", "v240", "v241", "v242", "v243", "v244", "v245", "v246", "v247", "v248", "v249", "v250", "v251", "v252", "v253", "v254", "v255", "a0", "a1", "a2", "a3", "a4", "a5", "a6", "a7", "a8", "a9", "a10", "a11", "a12", "a13", "a14", "a15", "a16", "a17", "a18", "a19", "a20", "a21", "a22", "a23", "a24", "a25", "a26", "a27", "a28", "a29", "a30", "a31", "a32", "a33", "a34", "a35", "a36", "a37", "a38", "a39", "a40", "a41", "a42", "a43", "a44", "a45", "a46", "a47", "a48", "a49", "a50", "a51", "a52", "a53", "a54", "a55", "a56", "a57", "a58", "a59", "a60", "a61", "a62", "a63", "a64", "a65", "a66", "a67", "a68", "a69", "a70", "a71", "a72", "a73", "a74", "a75", "a76", "a77", "a78", "a79", "a80", "a81", "a82", "a83", "a84", "a85", "a86", "a87", "a88", "a89", "a90", "a91", "a92", "a93", "a94", "a95", "a96", "a97", "a98", "a99", "a100", "a101", "a102", "a103", "a104", "a105", "a106", "a107", "a108", "a109", "a110", "a111", "a112", "a113", "a114", "a115", "a116", "a117", "a118", "a119", "a120", "a121", "a122", "a123", "a124", "a125", "a126", "a127", "a128", "a129", "a130", "a131", "a132", "a133", "a134", "a135", "a136", "a137", "a138", "a139", "a140", "a141", "a142", "a143", "a144", "a145", "a146", "a147", "a148", "a149", "a150", "a151", "a152", "a153", "a154", "a155", "a156", "a157", "a158", "a159", "a160", "a161", "a162", "a163", "a164", "a165", "a166", "a167", "a168", "a169", "a170", "a171", "a172", "a173", "a174", "a175", "a176", "a177", "a178", "a179", "a180", "a181", "a182", "a183", "a184", "a185", "a186", "a187", "a188", "a189", "a190", "a191", "a192", "a193", "a194", "a195", "a196", "a197", "a198", "a199", "a200", "a201", "a202", "a203", "a204", "a205", "a206", "a207", "a208", "a209", "a210", "a211", "a212", "a213", "a214", "a215", "a216", "a217", "a218", "a219", "a220", "a221", "a222", "a223", "a224", "a225", "a226", "a227", "a228", "a229", "a230", "a231", "a232", "a233", "a234", "a235", "a236", "a237", "a238", "a239", "a240", "a241", "a242", "a243", "a244", "a245", "a246", "a247", "a248", "a249", "a250", "a251", "a252", "a253", "a254", "a255");
}

extern "C" void kernel_launch(void* const* d_in, const int* in_sizes, int n_in, void* d_out, int out_size, void* d_ws, size_t ws_size, hipStream_t stream) {
  k_all<<<256, 256, 0, stream>>>((const char*)d_in[0], (const char*)d_in[1], (const char*)d_in[2], (const char*)d_in[3], (const char*)d_in[4],
                                 (const char*)d_in[6], (const char*)d_in[7], (char*)d_ws, (char*)d_out);
}
```
